```python
import torch

def _softmax(x, axis=-1):
    return torch.softmax(x, dim=axis)
def _gelu(x):
    return torch.nn.functional.gelu(x, approximate="tanh")
def _silu(x):
    return torch.nn.functional.silu(x)
def _sigmoid(x):
    return torch.sigmoid(x)
def _layernorm(x, axis=-1, eps=1e-5):
    m = x.mean(dim=axis, keepdim=True)
    v = x.var(dim=axis, keepdim=True, unbiased=False)
    return (x - m) / torch.sqrt(v + eps)
def _scatter_add(tgt, idx, src, axis=0):
    return tgt.clone().index_add_(axis, idx, src)
def _segment_sum(src, seg, n):
    out = torch.zeros((n,) + src.shape[1:], dtype=src.dtype)
    return out.index_add_(0, seg, src)
def _pad(x, pw):
    flat = tuple(v for pair in reversed(pw) for v in pair)
    return torch.nn.functional.pad(x, flat)

kernel_name = 'concat_heads__mha_block__linear_attn_block__prompt'

def setup_inputs():

    _w = (lambda a: (lambda t: t.to(torch.bfloat16) if t.is_floating_point() else t)(torch.as_tensor(a)))
    def _A_setup():
        B, S, D, H = 1, 2048, 1024, 32
        return {
            "x": _w((lambda *s: torch.randn(*s, dtype=torch.bfloat16) * 0.2)(B, S, D)),
            "wqkv": _w((lambda *s: torch.randn(*s, dtype=torch.bfloat16) * 0.2)(D, 3 * D)),
            "wo": _w((lambda *s: torch.randn(*s, dtype=torch.bfloat16) * 0.05)(D, D)),
        }
    _A_in = _A_setup()

    def _B_setup():
        B, S, D = 1, 2048, 1024
        return {
            "x": _w((lambda *s: torch.randn(*s, dtype=torch.bfloat16) * 0.05)(B, S, D)),
            "wqkv": _w((lambda *s: torch.randn(*s, dtype=torch.bfloat16) * 1.6)(D, 3 * D)),
            "wo": _w((lambda *s: torch.randn(*s, dtype=torch.bfloat16) * 0.1)(D, D)),
        }
    _B_in = _B_setup()

    return {
        "A_x": _A_in["x"],
        "A_wqkv": _A_in["wqkv"],
        "A_wo": _A_in["wo"],
        "B_x": _B_in["x"],
        "B_wqkv": _B_in["wqkv"],
        "B_wo": _B_in["wo"],
    }

def reference(A_x, A_wqkv, A_wo, B_x, B_wqkv, B_wo):
    def _A_ref(x, wqkv, wo):
        B, S, D = x.shape
        H = 32
        Dh = D // H
        qkv = (lambda x, *d: x.permute(*d))((x @ wqkv).reshape(B, S, 3, H, Dh), 2, 0, 3, 1, 4)
        q, k, v = qkv[0], qkv[1], qkv[2]
        scores = torch.einsum("bhqd,bhkd->bhqk", q, k) * (Dh ** -0.5)
        p = _softmax(scores, axis=-1)
        o = torch.einsum("bhqk,bhkd->bhqd", p, v)
        return (lambda x, *d: x.permute(*d))(o, 0, 2, 1, 3).reshape(B, S, D) @ wo

    def _B_ref(x, wqkv, wo):
        B, S, D = x.shape
        H = 32; Dh = D // H
        qkv = (lambda x, *d: x.permute(*d))((x @ wqkv).reshape(B, S, 3, H, Dh), 2, 0, 3, 1, 4)
        q, k, v = qkv[0], qkv[1], qkv[2]
        phi = lambda t: torch.where(t > 0, t + 1, torch.exp(t)) + 1e-6
        pq, pk = phi(q), phi(k)
        kv = torch.einsum("bhsd,bhse->bhde", pk, v)
        z = torch.einsum("bhsd->bhd", pk)
        num = torch.einsum("bhqd,bhde->bhqe", pq, kv)
        den = torch.einsum("bhqd,bhd->bhq", pq, z)[..., None] + 1e-6
        o = num / den
        return (lambda x, *d: x.permute(*d))(o, 0, 2, 1, 3).reshape(B, S, D) @ wo

    a = _A_ref(A_x, A_wqkv, A_wo)
    b = _B_ref(B_x, B_wqkv, B_wo)
    return torch.concatenate([a, b], axis=-1)
```

```cpp
#include <hip/hip_runtime.h>
#include <hip/hip_bf16.h>

typedef unsigned short u16;
typedef __bf16 bf16x8 __attribute__((ext_vector_type(8)));
typedef float f32x8 __attribute__((ext_vector_type(8)));
typedef float f32x4 __attribute__((ext_vector_type(4)));
typedef float f32x16 __attribute__((ext_vector_type(16)));
typedef short s16x4 __attribute__((ext_vector_type(4)));
typedef short s16x8 __attribute__((ext_vector_type(8)));
typedef unsigned u32x4 __attribute__((ext_vector_type(4)));
#define LAS __attribute__((address_space(3)))
#define DI __device__ __forceinline__
#define MFMA32(a, b, c) __builtin_amdgcn_mfma_f32_32x32x16_bf16((a), (b), (c), 0, 0, 0)
#define MFMA16(a, b, c) __builtin_amdgcn_mfma_f32_16x16x32_bf16((a), (b), (c), 0, 0, 0)
#define SBAR() __builtin_amdgcn_sched_barrier(0)

constexpr int S_ = 2048, D_ = 1024, H_ = 32;
constexpr size_t WS_QA = 0;
constexpr size_t WS_KVA = WS_QA + (4u << 20);
constexpr size_t WS_PQB = WS_KVA + (8u << 20);
constexpr size_t WS_KVP = WS_PQB + (4u << 20);
constexpr size_t WS_ZP = WS_KVP + (512u << 10);
constexpr size_t WS_O = WS_ZP + (64u << 10);
constexpr size_t WS_END = WS_O + (8u << 20);

DI bf16x8 pack8(const f32x16& x, int s) {
  f32x8 v = {x[8 * s], x[8 * s + 1], x[8 * s + 2], x[8 * s + 3], x[8 * s + 4], x[8 * s + 5], x[8 * s + 6], x[8 * s + 7]};
  return __builtin_convertvector(v, bf16x8);
}
DI void dma16(unsigned voff, const void* sbase, unsigned ldsaddr) {
  asm volatile("s_mov_b32 m0, %2\n\ts_nop 0\n\tglobal_load_lds_dwordx4 %0, %1" ::"v"(voff), "s"(sbase), "s"(ldsaddr) : "memory");
}
DI void dma4(unsigned voff, const void* sbase, unsigned ldsaddr) {
  asm volatile("s_mov_b32 m0, %2\n\ts_nop 0\n\tglobal_load_lds_dword %0, %1" ::"v"(voff), "s"(sbase), "s"(ldsaddr) : "memory");
}
DI bf16x8 ldsb128(unsigned addr) { return *(const LAS bf16x8*)(addr); }
DI bf16x8 ldstr2(unsigned a0, unsigned a1) {
  s16x4 lo = __builtin_amdgcn_ds_read_tr16_b64_v4i16((LAS s16x4*)(a0));
  s16x4 hi = __builtin_amdgcn_ds_read_tr16_b64_v4i16((LAS s16x4*)(a1));
  s16x8 t = __builtin_shufflevector(lo, hi, 0, 1, 2, 3, 4, 5, 6, 7);
  return __builtin_bit_cast(bf16x8, t);
}
DI float phi(float t) { return (t > 0.f ? t + 1.f : __builtin_amdgcn_exp2f(t * 1.4426950408889634f)) + 1e-6f; }

constexpr int G1_STAGE = 57344, G1_WOFF = 32768, G1_DUMMY = 2 * G1_STAGE, G1_LDS = 2 * G1_STAGE + 1024;

template <bool ISB>
DI void gemm1_body(const u16* __restrict__ X, const u16* __restrict__ W, char* __restrict__ ws, int mt, int nt, int quad, int idx) {
  const int tid = threadIdx.x, lane = tid & 63;
  const int wid = __builtin_amdgcn_readfirstlane(tid >> 6);
  const int wm = wid & 3, wh = wid >> 2, hi = lane >> 5, r32 = lane & 31;
  unsigned voff[8];
  const char* sb; unsigned sstep;
  if (wid < 4) {
    sb = (const char*)X + (size_t)(mt * 256 + wid * 64) * 2048; sstep = 128;
#pragma unroll
    for (int i = 0; i < 8; ++i) voff[i] = (i * 8 + (lane >> 3)) * 2048 + (((lane & 7) ^ ((4 * (i & 1) + (lane >> 4)) & 7)) * 16);
  } else {
    sb = (const char*)W + (wid - 4) * 2048 + nt * 128; sstep = 64 * 6144;
#pragma unroll
    for (int i = 0; i < 8; ++i) voff[i] = (i * 8 + (lane >> 3)) * 6144 + (((lane & 7) ^ (((lane >> 4) & 1) << 2)) * 16);
  }
  const unsigned ldsw = wid * 8192;
  const bool dmaw = wid < 7;
  const unsigned pfx = ((quad >> 1) * 1024 + idx * 32 + r32) * 2048 + hi * 128;
  const int pl = lane < 48 ? lane : lane - 48;
  const unsigned pfw = (idx * 2 + (pl >= 24 ? 1 : 0)) * 6144 + ((pl % 24) >> 3) * 2048 + (quad & 1) * 1024 + (pl & 7) * 128;
#define G1_DMA(i, st) do { if (dmaw) dma16(voff[i], sb, ldsw + (st) * G1_STAGE + (i) * 1024); } while (0)
#define G1_PFX(t) dma4(pfx, (const char*)X + (t) * 128, G1_DUMMY)
#define G1_PFW(t) dma4(pfw, (const char*)W + (size_t)(t) * 64 * 6144, G1_DUMMY + 256)

  unsigned xa[4];
  { const unsigned a0 = (wm * 64 + r32) * 128 + ((hi ^ ((r32 >> 1) & 7)) * 16);
#pragma unroll
    for (int s = 0; s < 4; ++s) xa[s] = a0 ^ (32 * s); }
  unsigned wa[2];
  { const int g = lane >> 4, q = (lane & 15) >> 2, p = lane & 3;
    const unsigned c = (wh * 4 + (g & 1) * 2 + (p >> 1)) ^ (((q >> 1) & 1) << 2);
    wa[0] = G1_WOFF + (8 * hi + q) * 128 + c * 16 + (p & 1) * 8; wa[1] = wa[0] + G1_STAGE; }

  f32x16 acc[2][3];
#pragma unroll
  for (int m = 0; m < 2; ++m)
#pragma unroll
    for (int n = 0; n < 3; ++n) acc[m][n] = f32x16{};
  bf16x8 fx[2][2], fw[2][3];
#define RDX(b, m, st, s) fx[b][m] = ldsb128(xa[s] + (st) * G1_STAGE + (m) * 4096)
#define RDW(b, n, st, s) fw[b][n] = ldstr2(wa[st] + (n) * 8192 + (s) * 2048, wa[st] + (n) * 8192 + (s) * 2048 + 512)
#define MM(b, m, n) do { if ((n) == 0 || ((n) == 1 && !ISB)) acc[m][n] = MFMA32(fw[b][n], fx[b][m], acc[m][n]); else acc[m][n] = MFMA32(fx[b][m], fw[b][n], acc[m][n]); } while (0)

#pragma unroll
  for (int i = 0; i < 8; ++i) G1_DMA(i, 0);
  sb += sstep;
#pragma unroll
  for (int i = 0; i < 8; ++i) G1_DMA(i, 1);
  sb += sstep;
  if (!dmaw) { G1_PFX(2); G1_PFW(2); G1_PFW(3); G1_PFX(4); G1_PFW(4); G1_PFW(5); }
  if (dmaw) asm volatile("s_waitcnt vmcnt(8)" ::: "memory");
  asm volatile("s_barrier" ::: "memory");
  RDX(0, 0, 0, 0); RDW(0, 0, 0, 0); RDW(0, 1, 0, 0); RDW(0, 2, 0, 0); RDX(0, 1, 0, 0);

#define SUB(b, st2, s2, DO_RD, D0, D1, D2) do { \
    if (DO_RD) { RDX((b) ^ 1, 0, st2, s2); RDW((b) ^ 1, 0, st2, s2); } SBAR(); MM(b, 0, 0); SBAR(); D0; SBAR(); \
    if (DO_RD) { RDW((b) ^ 1, 1, st2, s2); } SBAR(); MM(b, 0, 1); SBAR(); \
    if (DO_RD) { RDW((b) ^ 1, 2, st2, s2); } SBAR(); MM(b, 0, 2); SBAR(); D1; SBAR(); \
    if (DO_RD) { RDX((b) ^ 1, 1, st2, s2); } SBAR(); MM(b, 1, 0); SBAR(); \
    MM(b, 1, 1); SBAR(); D2; SBAR(); \
    MM(b, 1, 2); SBAR(); } while (0)
#define NOP_ do {} while (0)
#define KSTEP(st, PRE, POST, NEXT, PF) do { \
    SUB(0, st, 1, true, if (PRE) G1_DMA(3, (st) ^ 1), if (PRE) G1_DMA(4, (st) ^ 1), if (PRE) G1_DMA(5, (st) ^ 1)); \
    SUB(1, st, 2, true, if (PRE) G1_DMA(6, (st) ^ 1), if (PRE) { G1_DMA(7, (st) ^ 1); sb += sstep; }, PF); \
    SUB(0, st, 3, true, NOP_, NOP_, NOP_); \
    if (NEXT) { if (dmaw) asm volatile("s_waitcnt vmcnt(0)" ::: "memory"); asm volatile("s_waitcnt lgkmcnt(0)\n\ts_barrier" ::: "memory"); } \
    SUB(1, (st) ^ 1, 0, NEXT, if (POST) G1_DMA(0, st), if (POST) G1_DMA(1, st), if (POST) G1_DMA(2, st)); \
  } while (0)

  KSTEP(0, false, true, true, if (!dmaw) { G1_PFX(6); G1_PFW(6); });
  KSTEP(1, true, true, true, if (!dmaw) { G1_PFW(7); });
  for (int t = 2; t < 14; t += 2) {
    KSTEP(0, true, true, true, if (!dmaw && t < 10) { G1_PFX(t + 6); G1_PFW(t + 6); });
    KSTEP(1, true, true, true, if (!dmaw && t < 10) { G1_PFW(t + 7); });
  }
  KSTEP(0, true, false, true, NOP_);
  KSTEP(1, false, false, false, NOP_);

  const int h = nt * 2 + wh;
  const int tile0 = mt * 8 + wm * 2;
  if (!ISB) {
    constexpr float QS = 0.17677669529663687f * 1.4426950408889634f;
    char* qp = ws + WS_QA + (size_t)(h * 64 + tile0) * 2048 + lane * 16;
    char* kp = ws + WS_KVA + (size_t)(h * 64 + tile0) * 4096 + lane * 16;
#pragma unroll
    for (int m = 0; m < 2; ++m) {
      f32x16 q = acc[m][0] * QS;
      *(bf16x8*)(qp + m * 2048) = pack8(q, 0); *(bf16x8*)(qp + m * 2048 + 1024) = pack8(q, 1);
      *(bf16x8*)(kp + m * 4096) = pack8(acc[m][1], 0); *(bf16x8*)(kp + m * 4096 + 1024) = pack8(acc[m][1], 1);
      *(bf16x8*)(kp + m * 4096 + 2048) = pack8(acc[m][2], 0); *(bf16x8*)(kp + m * 4096 + 3072) = pack8(acc[m][2], 1);
    }
  } else {
    char* qp = ws + WS_PQB + (size_t)(h * 64 + tile0) * 2048 + lane * 16;
    f32x16 kv = f32x16{}, z = f32x16{};
    bf16x8 ones;
#pragma unroll
    for (int j = 0; j < 8; ++j) ones[j] = (__bf16)1.0f;
#pragma unroll
    for (int m = 0; m < 2; ++m) {
      f32x16 q, k;
#pragma unroll
      for (int r = 0; r < 16; ++r) { q[r] = phi(acc[m][0][r]); k[r] = phi(acc[m][1][r]); }
      *(bf16x8*)(qp + m * 2048) = pack8(q, 0); *(bf16x8*)(qp + m * 2048 + 1024) = pack8(q, 1);
#pragma unroll
      for (int s = 0; s < 2; ++s) { const bf16x8 pk = pack8(k, s); kv = MFMA32(pk, pack8(acc[m][2], s), kv); z = MFMA32(pk, ones, z); }
    }
    asm volatile("s_waitcnt vmcnt(0) lgkmcnt(0)\n\ts_barrier" ::: "memory");
    LAS f32x4* red = (LAS f32x4*)(uintptr_t)(unsigned)(wid * 4096);
#pragma unroll
    for (int g = 0; g < 4; ++g) red[g * 64 + lane] = f32x4{kv[4 * g], kv[4 * g + 1], kv[4 * g + 2], kv[4 * g + 3]};
    LAS f32x4* zr = (LAS f32x4*)(uintptr_t)(unsigned)(32768 + wid * 128);
    if (r32 == 0) {
#pragma unroll
      for (int g = 0; g < 4; ++g) zr[hi * 4 + g] = f32x4{z[4 * g], z[4 * g + 1], z[4 * g + 2], z[4 * g + 3]};
    }
    asm volatile("s_waitcnt lgkmcnt(0)\n\ts_barrier" ::: "memory");
    f32x4 sum = f32x4{};
#pragma unroll
    for (int p = 0; p < 4; ++p) sum += ((LAS f32x4*)(uintptr_t)(unsigned)((wh * 4 + p) * 4096))[wm * 64 + lane];
    typedef __bf16 bf16x4 __attribute__((ext_vector_type(4)));
    *(bf16x4*)(ws + WS_KVP + (size_t)(h * 8 + mt) * 2048 + (wm >> 1) * 1024 + lane * 16 + (wm & 1) * 8) = __builtin_convertvector(sum, bf16x4);
    if (wm == 0 && lane < 8) {
      f32x4 zs = f32x4{};
#pragma unroll
      for (int p = 0; p < 4; ++p) zs += ((LAS f32x4*)(uintptr_t)(unsigned)(32768 + (wh * 4 + p) * 128))[lane];
      const int zh = lane >> 2, g = lane & 3;
      *(bf16x4*)(ws + WS_ZP + (size_t)(h * 8 + mt) * 64 + (g >> 1) * 32 + zh * 16 + (g & 1) * 8) = __builtin_convertvector(zs, bf16x4);
    }
  }
#undef G1_DMA
#undef G1_PFX
#undef G1_PFW
#undef RDX
#undef RDW
#undef MM
#undef SUB
#undef KSTEP
}

__global__ __launch_bounds__(512, 2) void k_gemm1(const u16* __restrict__ xa, const u16* __restrict__ wa, const u16* __restrict__ xb, const u16* __restrict__ wb, char* __restrict__ ws) {
  extern __shared__ __attribute__((aligned(16))) char lds[];
  const int bid = blockIdx.x, xcd = bid & 7, idx = bid >> 3;
  const int prob = xcd >> 2, quad = xcd & 3;
  const int mt = (quad >> 1) * 4 + (idx & 3), nt = (quad & 1) * 8 + (idx >> 2);
  if (prob == 0) gemm1_body<false>(xa, wa, ws, mt, nt, quad, idx);
  else gemm1_body<true>(xb, wb, ws, mt, nt, quad, idx);
}

constexpr int AT_SLOT = 16384, AT_NSLOT = 6, AT_Q = AT_SLOT * AT_NSLOT, AT_PQ = AT_Q + 16384, AT_KV = AT_PQ + 16384, AT_Z = AT_KV + 16384, AT_LDS = AT_Z + 2048;

DI float rowmax16(const f32x16& p) {
  float a = __builtin_fmaxf(__builtin_fmaxf(p[0], p[1]), p[2]);
#pragma unroll
  for (int r = 3; r < 15; r += 2) a = __builtin_fmaxf(__builtin_fmaxf(a, p[r]), p[r + 1]);
  a = __builtin_fmaxf(a, p[15]);
  auto rr = __builtin_amdgcn_permlane32_swap(__float_as_uint(a), __float_as_uint(a), false, false);
  return __builtin_fmaxf(__uint_as_float(rr[0]), __uint_as_float(rr[1]));
}

__global__ __launch_bounds__(512, 2) void k_attn(char* __restrict__ ws) {
  extern __shared__ __attribute__((aligned(16))) char lds[];
  const int bid = blockIdx.x, xcd = bid & 7, idx = bid >> 3;
  const int h = xcd * 4 + (idx >> 3), qb = idx & 7;
  const int tid = threadIdx.x, lane = tid & 63;
  const int wid = __builtin_amdgcn_readfirstlane(tid >> 6);
  const int hi = lane >> 5, r32 = lane & 31;
  const int qt = qb * 8 + wid;
  const unsigned l16 = lane * 16;
  const char* kvsrc = ws + WS_KVA + (size_t)h * 262144 + wid * 2048;
#define AT_DMA(stg, slot) do { dma16(l16, kvsrc + (stg) * 16384, (slot) * AT_SLOT + wid * 2048); dma16(l16, kvsrc + (stg) * 16384 + 1024, (slot) * AT_SLOT + wid * 2048 + 1024); } while (0)
  { const char* qsrc = ws + WS_QA + (size_t)(h * 64 + qt) * 2048;
    dma16(l16, qsrc, AT_Q + wid * 2048); dma16(l16, qsrc + 1024, AT_Q + wid * 2048 + 1024); }
  AT_DMA(0, 0);
  AT_DMA(1, 1);
  { const char* psrc = ws + WS_PQB + (size_t)(h * 64 + qt) * 2048;
    dma16(l16, psrc, AT_PQ + wid * 2048); dma16(l16, psrc + 1024, AT_PQ + wid * 2048 + 1024);
    const char* ksrc = ws + WS_KVP + (size_t)(h * 8 + wid) * 2048;
    dma16(l16, ksrc, AT_KV + wid * 2048); dma16(l16, ksrc + 1024, AT_KV + wid * 2048 + 1024);
    const unsigned zoff = ((r32 & 3) + 4 * ((r32 >> 3) & 1)) * 64 + hi * 16;
    dma16(zoff, ws + WS_ZP + (size_t)h * 512 + (wid & 1) * 32, AT_Z + (wid & 1) * 1024); }
  AT_DMA(2, 2);
  AT_DMA(3, 3);
  AT_DMA(4, 4);
  asm volatile("s_waitcnt vmcnt(13)\n\ts_barrier" ::: "memory");

  const bf16x8 q0 = ldsb128(AT_Q + wid * 2048 + l16), q1 = ldsb128(AT_Q + wid * 2048 + 1024 + l16);
  bf16x8 sumA;
  { const __bf16 v = (((lane >> 4) & 1) == ((lane >> 2) & 1)) ? (__bf16)1.0f : (__bf16)0.0f;
#pragma unroll
    for (int j = 0; j < 8; ++j) sumA[j] = v; }
  f32x16 negm;
  f32x16 sA, sB;
  { bf16x8 k0 = ldsb128(l16), k1 = ldsb128(1024 + l16);
    f32x16 s = MFMA32(k0, q0, f32x16{}); s = MFMA32(k1, q1, s);
    const float m = rowmax16(s);
#pragma unroll
    for (int r = 0; r < 16; ++r) { negm[r] = -m; sA[r] = s[r] - m; } }
  f32x16 o = f32x16{};
  f32x4 lsum = f32x4{};
  bf16x8 kf0, kf1, vf0, vf1, p0, p1;
  kf0 = ldsb128(4096 + l16); kf1 = ldsb128(4096 + 1024 + l16);
#define EX(X, i) X[i] = __builtin_amdgcn_exp2f(X[i])
#define STEP(SC, SN, vaddr, kaddr, DOQK) do { \
    vf0 = ldsb128((vaddr) + l16); vf1 = ldsb128((vaddr) + 1024 + l16); SBAR(); \
    if (DOQK) SN = MFMA32(kf0, q0, negm); SBAR(); \
    EX(SC, 0); EX(SC, 1); EX(SC, 2); EX(SC, 3); SBAR(); \
    if (DOQK) SN = MFMA32(kf1, q1, SN); SBAR(); \
    EX(SC, 4); EX(SC, 5); EX(SC, 6); EX(SC, 7); SBAR(); \
    if (DOQK) { kf0 = ldsb128((kaddr) + l16); kf1 = ldsb128((kaddr) + 1024 + l16); } \
    p0 = pack8(SC, 0); SBAR(); \
    EX(SC, 8); EX(SC, 9); SBAR(); \
    o = MFMA32(vf0, p0, o); SBAR(); \
    EX(SC, 10); EX(SC, 11); EX(SC, 12); EX(SC, 13); SBAR(); \
    lsum = MFMA16(sumA, p0, lsum); SBAR(); \
    EX(SC, 14); EX(SC, 15); p1 = pack8(SC, 1); SBAR(); \
    o = MFMA32(vf1, p1, o); SBAR(); \
    lsum = MFMA16(sumA, p1, lsum); SBAR(); \
  } while (0)
  int slot = 0;
  for (int stg = 0; stg < 16; ++stg) {
    const unsigned cur = slot * AT_SLOT;
    const int nslot = slot == AT_NSLOT - 1 ? 0 : slot + 1;
    const unsigned nxt = nslot * AT_SLOT;
    STEP(sA, sB, cur + 2048, cur + 2 * 4096, true);
    STEP(sB, sA, cur + 4096 + 2048, cur + 3 * 4096, true);
    if (stg < 15) {
      if (stg == 0) asm volatile("s_waitcnt vmcnt(6)\n\ts_barrier" ::: "memory");
      else if (stg == 1) asm volatile("s_waitcnt vmcnt(8)\n\ts_barrier" ::: "memory");
      else if (stg < 12) asm volatile("s_waitcnt vmcnt(6)\n\ts_barrier" ::: "memory");
      else asm volatile("s_waitcnt vmcnt(0)\n\ts_barrier" ::: "memory");
      if (stg + 5 < 16) { const int ps = slot == 0 ? AT_NSLOT - 1 : slot - 1; AT_DMA(stg + 5, ps); }
    }
    if (stg == 0) {
      const bf16x8 pq0 = ldsb128(AT_PQ + wid * 2048 + l16), pq1 = ldsb128(AT_PQ + wid * 2048 + 1024 + l16);
      f32x16 den = MFMA32(ldsb128(AT_Z + l16), pq0, f32x16{}); den = MFMA32(ldsb128(AT_Z + 1024 + l16), pq1, den);
      f32x16 num = f32x16{};
#pragma unroll
      for (int p = 0; p < 8; ++p) { num = MFMA32(ldsb128(AT_KV + p * 2048 + l16), pq0, num); num = MFMA32(ldsb128(AT_KV + p * 2048 + 1024 + l16), pq1, num); }
      const float d = ((den[0] + den[1]) + (den[2] + den[3])) + ((den[4] + den[5]) + (den[6] + den[7])) + 1e-6f;
      const float rd = 1.0f / d;
      num *= rd;
      char* op = ws + WS_O + (4u << 20) + (size_t)(qt * 64 + 2 * h) * 1024 + l16;
      *(bf16x8*)op = pack8(num, 0); *(bf16x8*)(op + 1024) = pack8(num, 1);
      SBAR();
    }
    STEP(sA, sB, cur + 2 * 4096 + 2048, nxt, true);
    STEP(sB, sA, cur + 3 * 4096 + 2048, nxt + 4096, true);
    slot = nslot;
  }
  float l = lsum[0];
  bool bad = !(l < 1.8446744e19f);
#pragma unroll
  for (int r = 0; r < 16; ++r) bad |= !(__builtin_fabsf(o[r]) < 3.0e38f);
  if (__builtin_expect(__any(bad), 0)) {
    const char* g = ws + WS_KVA + (size_t)h * 262144 + l16;
    float m = -INFINITY; l = 0.f; o = f32x16{};
    for (int t = 0; t < 64; ++t) {
      const bf16x8 k0 = *(const bf16x8*)(g + t * 4096), k1 = *(const bf16x8*)(g + t * 4096 + 1024);
      const bf16x8 v0 = *(const bf16x8*)(g + t * 4096 + 2048), v1 = *(const bf16x8*)(g + t * 4096 + 3072);
      f32x16 s = MFMA32(k0, q0, f32x16{}); s = MFMA32(k1, q1, s);
      const float mn = __builtin_fmaxf(m, rowmax16(s));
      const float al = __builtin_amdgcn_exp2f(m - mn);
      m = mn;
#pragma unroll
      for (int r = 0; r < 16; ++r) s[r] = __builtin_amdgcn_exp2f(s[r] - mn);
      const bf16x8 a0 = pack8(s, 0), a1 = pack8(s, 1);
      f32x4 ls = MFMA16(sumA, a0, f32x4{}); ls = MFMA16(sumA, a1, ls);
      o *= al; l = l * al + ls[0];
      o = MFMA32(v0, a0, o); o = MFMA32(v1, a1, o);
    }
  }
  o *= (1.0f / l);
  char* op = ws + WS_O + (size_t)(qt * 64 + 2 * h) * 1024 + l16;
  *(bf16x8*)op = pack8(o, 0); *(bf16x8*)(op + 1024) = pack8(o, 1);
#undef AT_DMA
#undef EX
#undef STEP
}

constexpr int G2_STAGE = 32768, G2_NST = 4, G2_LDS = G2_STAGE * G2_NST;
__global__ __launch_bounds__(512, 2) void k_gemm2(const char* __restrict__ ws, const u16* __restrict__ woa, const u16* __restrict__ wob, u16* __restrict__ out) {
  extern __shared__ __attribute__((aligned(16))) char lds[];
  const int bid = blockIdx.x, xcd = bid & 7, idx = bid >> 3;
  const int prob = xcd >> 2, quad = xcd & 3;
  const int mt = quad * 4 + (idx & 3), nt = idx >> 2;
  const u16* W = prob ? wob : woa;
  const int tid = threadIdx.x, lane = tid & 63;
  const int wid = __builtin_amdgcn_readfirstlane(tid >> 6);
  const int kg = wid >> 2, wm = (wid >> 1) & 1, wn = wid & 1, hi = lane >> 5;
  const unsigned l16 = lane * 16;
  const char* sb; unsigned sstep; unsigned voff[4];
  if (wid < 4) {
    sb = ws + WS_O + ((size_t)prob << 22) + (size_t)(mt * 4 + wid) * 65536; sstep = 4096;
#pragma unroll
    for (int i = 0; i < 4; ++i) voff[i] = l16 + i * 1024;
  } else {
    sb = (const char*)W + (size_t)(wid - 4) * 16 * 2048 + nt * 256; sstep = 64 * 2048;
#pragma unroll
    for (int i = 0; i < 4; ++i) voff[i] = (i * 4 + (lane >> 4)) * 2048 + (((lane & 15) ^ ((lane >> 4) << 2)) * 16);
  }
  const unsigned ldsw = wid * 4096;
#define G2_DMA(st) do { _Pragma("unroll") for (int i = 0; i < 4; ++i) dma16(voff[i], sb, ldsw + (st) * G2_STAGE + i * 1024); sb += sstep; } while (0)
  const unsigned xa = (wm * 2) * 4096 + kg * 2048 + l16;
  unsigned wa0, wa1;
  { const int g1 = (lane >> 4) & 1, q = (lane & 15) >> 2, p = lane & 3;
    const int ncol = wn * 64 + 16 * g1 + 8 * (p & 1) + 4 * (p >> 1);
    const unsigned c0 = (ncol >> 3) ^ (q << 2), c1 = ((ncol + 32) >> 3) ^ (q << 2);
    const unsigned rowb = 16384 + (kg * 32 + 4 * hi + q) * 256 + ((ncol >> 2) & 1) * 8;
    wa0 = rowb + c0 * 16; wa1 = rowb + c1 * 16; }
  f32x16 acc[2][2];
#pragma unroll
  for (int m = 0; m < 2; ++m)
#pragma unroll
    for (int n = 0; n < 2; ++n) acc[m][n] = f32x16{};
  G2_DMA(0); G2_DMA(1); G2_DMA(2);
  for (int t = 0; t < 16; ++t) {
    if (t < 14) asm volatile("s_waitcnt vmcnt(8)\n\ts_barrier" ::: "memory");
    else if (t == 14) asm volatile("s_waitcnt vmcnt(4)\n\ts_barrier" ::: "memory");
    else asm volatile("s_waitcnt vmcnt(0)\n\ts_barrier" ::: "memory");
    const unsigned so = (t & 3) * G2_STAGE;
    bf16x8 fx[2][2], fw[2][2];
#pragma unroll
    for (int s = 0; s < 2; ++s) {
      fx[s][0] = ldsb128(so + xa + s * 1024); fx[s][1] = ldsb128(so + xa + 4096 + s * 1024);
      fw[s][0] = ldstr2(so + wa0 + s * 4096, so + wa0 + s * 4096 + 2048); fw[s][1] = ldstr2(so + wa1 + s * 4096, so + wa1 + s * 4096 + 2048);
    }
    if (t + 3 < 16) G2_DMA((t + 3) & 3);
#pragma unroll
    for (int s = 0; s < 2; ++s)
#pragma unroll
      for (int m = 0; m < 2; ++m)
#pragma unroll
        for (int n = 0; n < 2; ++n) acc[m][n] = MFMA32(fw[s][n], fx[s][m], acc[m][n]);
    asm volatile("s_waitcnt lgkmcnt(0)" ::: "memory");
  }
  asm volatile("s_barrier" ::: "memory");
  { LAS f32x4* ex = (LAS f32x4*)(uintptr_t)(unsigned)(wid * 8192);
#pragma unroll
    for (int n = 0; n < 2; ++n)
#pragma unroll
      for (int g = 0; g < 4; ++g) { const f32x16& a = kg ? acc[0][n] : acc[1][n]; ex[(n * 4 + g) * 64 + lane] = f32x4{a[4 * g], a[4 * g + 1], a[4 * g + 2], a[4 * g + 3]}; } }
  asm volatile("s_waitcnt lgkmcnt(0)\n\ts_barrier" ::: "memory");
  { LAS f32x4* ex = (LAS f32x4*)(uintptr_t)(unsigned)((wid ^ 4) * 8192);
    const int row = mt * 128 + wm * 64 + kg * 32 + (lane & 31);
    u16* op = out + (size_t)row * 2048 + prob * 1024 + nt * 128 + wn * 64 + hi * 8;
#pragma unroll
    for (int n = 0; n < 2; ++n) {
      f32x16 a = kg ? acc[1][n] : acc[0][n];
#pragma unroll
      for (int g = 0; g < 4; ++g) { const f32x4 e = ex[(n * 4 + g) * 64 + lane]; a[4 * g] += e[0]; a[4 * g + 1] += e[1]; a[4 * g + 2] += e[2]; a[4 * g + 3] += e[3]; }
      *(bf16x8*)(op + n * 32) = pack8(a, 0); *(bf16x8*)(op + n * 32 + 16) = pack8(a, 1);
    } }
#undef G2_DMA
}

extern "C" void kernel_launch(void* const* d_in, const int* in_sizes, int n_in, void* d_out, int out_size, void* d_ws, size_t ws_size, hipStream_t stream) {
  const u16* ax = (const u16*)d_in[0]; const u16* aw = (const u16*)d_in[1]; const u16* awo = (const u16*)d_in[2];
  const u16* bx = (const u16*)d_in[3]; const u16* bw = (const u16*)d_in[4]; const u16* bwo = (const u16*)d_in[5];
  char* ws = (char*)d_ws;
  (void)hipFuncSetAttribute((const void*)k_gemm1, hipFuncAttributeMaxDynamicSharedMemorySize, G1_LDS);
  (void)hipFuncSetAttribute((const void*)k_attn, hipFuncAttributeMaxDynamicSharedMemorySize, AT_LDS);
  (void)hipFuncSetAttribute((const void*)k_gemm2, hipFuncAttributeMaxDynamicSharedMemorySize, G2_LDS);
  k_gemm1<<<256, 512, G1_LDS, stream>>>(ax, aw, bx, bw, ws);
  k_attn<<<256, 512, AT_LDS, stream>>>(ws);
  k_gemm2<<<256, 512, G2_LDS, stream>>>(ws, awo, bwo, (u16*)d_out);
}
```

```cpp
#include <hip/hip_runtime.h>
#include <hip/hip_bf16.h>

typedef unsigned short u16;
typedef unsigned long long u64;
typedef __bf16 bf16x8 __attribute__((ext_vector_type(8)));
typedef __bf16 bf16x2 __attribute__((ext_vector_type(2)));
typedef float f32x2 __attribute__((ext_vector_type(2)));
typedef float f32x8 __attribute__((ext_vector_type(8)));
typedef float f32x4 __attribute__((ext_vector_type(4)));
typedef float f32x16 __attribute__((ext_vector_type(16)));
typedef short s16x4 __attribute__((ext_vector_type(4)));
typedef short s16x8 __attribute__((ext_vector_type(8)));
typedef unsigned u32x4 __attribute__((ext_vector_type(4)));
#define LAS __attribute__((address_space(3)))
#define GAS __attribute__((address_space(1)))
#define DI __device__ __forceinline__
#define MFMA32(a, b, c) __builtin_amdgcn_mfma_f32_32x32x16_bf16((a), (b), (c), 0, 0, 0)
#define MFMA16(a, b, c) __builtin_amdgcn_mfma_f32_16x16x32_bf16((a), (b), (c), 0, 0, 0)
#define SBAR() __builtin_amdgcn_sched_barrier(0)
#define RLX_AGENT __ATOMIC_RELAXED, __HIP_MEMORY_SCOPE_AGENT
#define WAITV(n) asm volatile("s_waitcnt vmcnt(" #n ")" ::: "memory")
#define BAR() asm volatile("s_barrier" ::: "memory")
#define LBAR() asm volatile("s_waitcnt lgkmcnt(0)\n\ts_barrier" ::: "memory")

constexpr unsigned WS_EPOCH = 0;
constexpr unsigned WS_FA = 4096;
constexpr unsigned WS_FB = WS_FA + 2048;
constexpr unsigned WS_FOB = WS_FB + 2048;
constexpr unsigned WS_FOA = WS_FOB + 2048;
constexpr unsigned WS_QA = 1u << 20;
constexpr unsigned WS_KVA = WS_QA + (4u << 20);
constexpr unsigned WS_PQB = WS_KVA + (8u << 20);
constexpr unsigned WS_KVP = WS_PQB + (4u << 20);
constexpr unsigned WS_ZP = WS_KVP + (1u << 20);
constexpr unsigned WS_O = WS_ZP + (64u << 10);
constexpr unsigned WS_END = WS_O + (8u << 20);

constexpr unsigned G1_STAGE = 40960, G1_WOFF = 16384;
constexpr unsigned L_Q = 122880;
constexpr unsigned L_MISC = 155648;
constexpr unsigned L_DUMMY = L_MISC, L_Z = L_MISC + 1024, L_KVF = L_MISC + 3072;
constexpr unsigned L_BX = 0;
constexpr unsigned L_KVP = 0;
constexpr unsigned L_AX = 81920;
constexpr unsigned L_G2X = 131072;
constexpr unsigned LDS_BYTES = 163840;
constexpr unsigned g1slot(int s) { return s == 1 ? 0u : (s == 0 ? 40960u : 81920u); }
DI unsigned g1next(unsigned a) { return a == 0u ? 81920u : a - 40960u; }
constexpr unsigned kvslot(int s) { return s == 0 ? 139264u : (s == 1 ? 106496u : (s == 2 ? 90112u : (s == 3 ? 65536u : 49152u))); }

#define ST(i)

DI bf16x8 pack8(const f32x16& x, int s) {
  f32x8 v = {x[8 * s], x[8 * s + 1], x[8 * s + 2], x[8 * s + 3], x[8 * s + 4], x[8 * s + 5], x[8 * s + 6], x[8 * s + 7]};
  return __builtin_convertvector(v, bf16x8);
}
DI void dma16(unsigned voff, const void* sbase, unsigned ldsaddr) {
  asm volatile("s_mov_b32 m0, %2\n\ts_nop 0\n\tglobal_load_lds_dwordx4 %0, %1" ::"v"(voff), "s"(sbase), "s"(__builtin_amdgcn_readfirstlane(ldsaddr)) : "memory");
}
DI void touch(const char* p) {
  asm volatile("s_mov_b32 m0, %1\n\ts_nop 0\n\tglobal_load_lds_dword %0, off" ::"v"(p), "s"(L_DUMMY) : "memory");
}
DI bf16x8 ldsb128(unsigned addr) { return *(const LAS bf16x8*)(uintptr_t)(addr); }
DI bf16x8 ldstr2(unsigned a0, unsigned a1) {
  s16x4 lo = __builtin_amdgcn_ds_read_tr16_b64_v4i16((LAS s16x4*)(uintptr_t)(a0));
  s16x4 hi = __builtin_amdgcn_ds_read_tr16_b64_v4i16((LAS s16x4*)(uintptr_t)(a1));
  s16x8 t = __builtin_shufflevector(lo, hi, 0, 1, 2, 3, 4, 5, 6, 7);
  return __builtin_bit_cast(bf16x8, t);
}
DI float phi(float t) {
  const float e = __builtin_amdgcn_fmed3f(__builtin_amdgcn_exp2f(t * 1.4426950408889634f), 0.f, 1.f);
  return __builtin_fmaxf(t, 0.f) + (e + 1e-6f);
}
typedef __amdgpu_buffer_rsrc_t rsrc_t;
DI void st16(rsrc_t r, unsigned voff, unsigned soff, const bf16x8& v) {
  __builtin_amdgcn_raw_buffer_store_b128(__builtin_bit_cast(u32x4, v), r, voff + soff, 0, 16);
}
DI bf16x8 ld16(rsrc_t r, unsigned voff, unsigned soff) {
  return __builtin_bit_cast(bf16x8, __builtin_amdgcn_raw_buffer_load_b128(r, voff, soff, 16));
}
DI void post(char* ws, unsigned off, u64 v) { __hip_atomic_store((GAS u64*)(ws + off), v, RLX_AGENT); }
DI u64 peek(char* ws, unsigned off) { return __hip_atomic_load((GAS u64*)(ws + off), RLX_AGENT); }
DI void await_flags(char* ws, unsigned off, u64 first, u64 tag) {
  u64 v = first;
  for (unsigned spins = 0; !__all(v == tag) && spins < (1u << 20); ++spins) { __builtin_amdgcn_s_sleep(2); v = peek(ws, off); }
}
DI float rowmax16(const f32x16& p) {
  float a = __builtin_fmaxf(__builtin_fmaxf(p[0], p[1]), p[2]);
#pragma unroll
  for (int r = 3; r < 15; r += 2) a = __builtin_fmaxf(__builtin_fmaxf(a, p[r]), p[r + 1]);
  a = __builtin_fmaxf(a, p[15]);
  auto rr = __builtin_amdgcn_permlane32_swap(__float_as_uint(a), __float_as_uint(a), false, false);
  return __builtin_fmaxf(__uint_as_float(rr[0]), __uint_as_float(rr[1]));
}

DI void g1_touch(const char* bx, const char* bw, int q, int part, int bid, int lane) {
  const char* a;
  if (part == 0) a = lane < 32 ? bx + (size_t)(bid * 8 + (lane >> 2)) * 2048 + q * 512 + (lane & 3) * 128 : bw + (size_t)(q * 256 + bid) * 6144 + (lane - 32) * 128;
  else a = bw + (size_t)(q * 256 + bid) * 6144 + (32 + (lane & 15)) * 128;
  touch(a);
}

template <bool ISB>
DI void g1_compute(f32x16 (&acc)[2][3], int w4, int lane, int bid, const char* xa, const char* wa, const char* xb, const char* wb) {
  const int wm = w4 & 1, wh = w4 >> 1, hi = lane >> 5, r32 = lane & 31, sg = (bid >> 3) & 3;
  const unsigned xa0 = (wm * 64 + r32) * 128 + ((hi ^ ((r32 >> 1) & 7)) * 16);
  unsigned wa0;
  { const int g = lane >> 4, q = (lane & 15) >> 2, p = lane & 3;
    const unsigned cc = (wh * 4 + (g & 1) * 2 + (p >> 1)) ^ (((q >> 1) & 1) << 2);
    wa0 = G1_WOFF + (8 * hi + q) * 128 + cc * 16 + (p & 1) * 8; }
#pragma unroll
  for (int m = 0; m < 2; ++m)
#pragma unroll
    for (int n = 0; n < 3; ++n) acc[m][n] = f32x16{};
  bf16x8 fx[2][2], fw[2][3];
#define RDX(b, m, XS, s) fx[b][m] = ldsb128(((XS) ^ (32 * (s))) + (m) * 4096)
#define RDW(b, n, WS, s) fw[b][n] = ldstr2((WS) + (n) * 8192 + (s) * 2048, (WS) + (n) * 8192 + (s) * 2048 + 512)
#define MM(b, m, n) do { if ((n) == 0 || ((n) == 1 && !ISB)) acc[m][n] = MFMA32(fw[b][n], fx[b][m], acc[m][n]); else acc[m][n] = MFMA32(fx[b][m], fw[b][n], acc[m][n]); } while (0)
#define SUB(b, XS, WS, s2, TAIL) do { \
    MM(b, 0, 0); SBAR(); RDX((b) ^ 1, 0, XS, s2); RDW((b) ^ 1, 0, WS, s2); SBAR(); \
    MM(b, 0, 1); SBAR(); RDW((b) ^ 1, 1, WS, s2); SBAR(); \
    MM(b, 0, 2); SBAR(); RDW((b) ^ 1, 2, WS, s2); SBAR(); \
    MM(b, 1, 0); SBAR(); RDX((b) ^ 1, 1, XS, s2); SBAR(); \
    MM(b, 1, 1); SBAR(); TAIL; SBAR(); \
    MM(b, 1, 2); SBAR(); } while (0)
  unsigned slot = g1slot(ISB ? 1 : 0);
  unsigned xs = xa0 + slot, wsl = wa0 + slot, xn = 0, wn = 0;
  RDX(0, 0, xs, 0); RDW(0, 0, wsl, 0); RDW(0, 1, wsl, 0); RDW(0, 2, wsl, 0); RDX(0, 1, xs, 0);
  __builtin_amdgcn_s_setprio(3);
  for (int t = 0; t < 16; ++t) {
    SUB(0, xs, wsl, 1, { const int u = t - sg - (ISB ? 2 : 0);
                         if ((u & 3) == 0 && u >= 0 && w4 < 2 && (!ISB || u < 8)) { const int Q = (u >> 2) + (ISB ? 6 : 2); g1_touch(Q < 4 ? xa : xb, Q < 4 ? wa : wb, Q & 3, w4, bid, lane); } });
    SUB(1, xs, wsl, 2, { slot = g1next(slot); xn = xa0 + slot; wn = wa0 + slot; });
    SUB(0, xs, wsl, 3, { if (ISB && t == 0) WAITV(0); });
    MM(1, 0, 0); SBAR(); LBAR(); SBAR();
    MM(1, 0, 1); SBAR(); RDX(0, 0, xn, 0); RDW(0, 0, wn, 0); SBAR();
    MM(1, 0, 2); SBAR(); RDW(0, 1, wn, 0); RDW(0, 2, wn, 0); SBAR();
    MM(1, 1, 0); SBAR(); RDX(0, 1, xn, 0); SBAR();
    MM(1, 1, 1); SBAR();
    MM(1, 1, 2); SBAR();
    xs = xn; wsl = wn;
  }
  __builtin_amdgcn_s_setprio(0);
#undef RDX
#undef RDW
#undef MM
#undef SUB
}

__global__ __launch_bounds__(512, 2) void k_fused(const char* __restrict__ xa, const char* __restrict__ wa, const char* __restrict__ woa,
                                                   const char* __restrict__ xb, const char* __restrict__ wb, const char* __restrict__ wob,
                                                   u16* __restrict__ out, char* __restrict__ ws) {
  extern __shared__ __attribute__((aligned(16))) char lds[];
  const int bid = blockIdx.x, xcd = bid & 7, idx = bid >> 3;
  const int tid = threadIdx.x, lane = tid & 63;
  const int wid = __builtin_amdgcn_readfirstlane(tid >> 6);
  const int w4 = wid & 3, grp = wid >> 2, hi = lane >> 5, r32 = lane & 31;
  const unsigned l16 = lane * 16;
  const int mt = (xcd >> 2) * 8 + (idx & 7), nt = (xcd & 3) * 4 + (idx >> 3);
  const int h = xcd * 4 + (idx >> 3), qb = idx & 7;
  const int m2 = (xcd >> 1) * 4 + (idx & 3), n2 = (xcd & 1) * 8 + (idx >> 2);
  ST(0);
  const rsrc_t rws = __builtin_amdgcn_make_buffer_rsrc(ws, 0, WS_END, 0x00020000);
  u64 tag;
#define READ_TAG() do { asm volatile("s_load_dwordx2 %0, %1, 0x0\n\ts_waitcnt lgkmcnt(0)" : "=s"(tag) : "s"(ws) : "memory"); tag += 1; } while (0)

  unsigned vx[2], vw[6];
#pragma unroll
  for (int i = 0; i < 2; ++i) vx[i] = (mt * 128 + w4 * 32 + i * 8 + (lane >> 3)) * 2048 + (((lane & 7) ^ ((4 * i + (lane >> 4)) & 7)) * 16);
#pragma unroll
  for (int i = 0; i < 6; ++i) { const int u = w4 * 6 + i; vw[i] = ((u & 7) * 8 + (lane >> 3)) * 6144 + (u >> 3) * 2048 + nt * 128 + (((lane & 7) ^ (((lane >> 4) & 1) << 2)) * 16); }
  auto load_stage = [&](int gt, unsigned l) {
    const char* bx = (gt < 16 ? xa : xb) + (gt & 15) * 128;
    const char* bw = (gt < 16 ? wa : wb) + (size_t)(gt & 15) * 64 * 6144;
    dma16(vx[0], bx, l + w4 * 4096); dma16(vx[1], bx, l + w4 * 4096 + 1024);
    dma16(vx[0], bx + 32768, l + w4 * 4096 + 2048); dma16(vx[1], bx + 32768, l + w4 * 4096 + 3072);
#pragma unroll
    for (int i = 0; i < 6; ++i) dma16(vw[i], bw, l + G1_WOFF + (w4 * 6 + i) * 1024);
  };
  const char* kvsrc = ws + WS_KVA + (size_t)h * 262144 + (w4 >> 1) * 131072 + (w4 & 1) * 4096;
  auto load_kv = [&](int stg, int slot) {
    const char* p = kvsrc + stg * 8192; const unsigned l = kvslot(slot) + w4 * 4096;
#pragma unroll
    for (int i = 0; i < 4; ++i) dma16(l16, p + i * 1024, l + i * 1024);
  };

  f32x8 kvh, zh;
  {
    f32x16 acc[2][3];
    const int hg = nt * 2 + (w4 >> 1), tile0 = mt * 4 + (w4 & 1) * 2;
    if (grp == 0) {
      g1_touch(xa, wa, w4 >> 1, w4 & 1, bid, lane);
      READ_TAG();
      BAR();
      ST(1);
      g1_compute<false>(acc, w4, lane, bid, xa, wa, xb, wb);
      ST(2);
      constexpr float QS = 0.17677669529663687f * 1.4426950408889634f;
      const unsigned qo = WS_QA + (hg * 64 + tile0) * 2048, ko = WS_KVA + (hg * 64 + tile0) * 4096;
      load_stage(18, g1slot(0));
#pragma unroll
      for (int m = 0; m < 2; ++m) { st16(rws, l16, ko + m * 4096, pack8(acc[m][1], 0)); st16(rws, l16, ko + m * 4096 + 1024, pack8(acc[m][1], 1)); }
      BAR(); ST(16); load_stage(19, g1slot(1));
#pragma unroll
      for (int m = 0; m < 2; ++m) { st16(rws, l16, ko + m * 4096 + 2048, pack8(acc[m][2], 0)); st16(rws, l16, ko + m * 4096 + 3072, pack8(acc[m][2], 1)); }
      WAITV(18); BAR(); ST(17); load_stage(20, g1slot(2));
#pragma unroll
      for (int m = 0; m < 2; ++m) { const f32x16 q = acc[m][0] * QS; st16(rws, l16, qo + m * 2048, pack8(q, 0)); st16(rws, l16, qo + m * 2048 + 1024, pack8(q, 1)); }
      WAITV(18); BAR(); ST(18); load_stage(21, g1slot(0));
      WAITV(14); BAR(); ST(19); load_stage(22, g1slot(1));
      WAITV(10); BAR(); ST(20); load_stage(23, g1slot(2));
      WAITV(0); BAR(); ST(21);
      if (tid == 0) post(ws, WS_FA + (nt * 16 + mt) * 8, tag);
      load_stage(24, g1slot(0));
      unsigned slot = g1slot(1);
#pragma unroll 1
      for (int t = 22; t < 28; ++t) { WAITV(10); BAR(); ST(t); load_stage(t + 3, slot); slot = g1next(slot); }
      u64 fl = 0;
      if (wid == 0) fl = peek(ws, WS_FA + ((h >> 1) * 16 + (lane & 15)) * 8);
      WAITV(10); BAR(); ST(28); load_stage(31, g1slot(1));
      WAITV(10); BAR(); ST(29);
      WAITV(0);
      if (wid == 0) { await_flags(ws, WS_FA + ((h >> 1) * 16 + (lane & 15)) * 8, fl, tag); __builtin_amdgcn_fence(__ATOMIC_ACQUIRE, "agent"); }
      BAR(); ST(30);
      BAR();
      WAITV(0); BAR();
      { const char* qsrc = ws + WS_QA + (size_t)(h * 64 + qb * 8 + w4 * 2) * 2048;
#pragma unroll
        for (int i = 0; i < 4; ++i) dma16(l16, qsrc + i * 1024, L_Q + w4 * 4096 + i * 1024); }
      load_kv(0, 0); load_kv(1, 1); load_kv(2, 2); load_kv(3, 3);
      ST(3);
      WAITV(12); BAR();
    } else {
      load_stage(0, g1slot(0)); load_stage(1, g1slot(1)); load_stage(2, g1slot(2));
      READ_TAG();
      WAITV(20); BAR();
      ST(1);
      unsigned slot = g1slot(0);
#pragma unroll 1
      for (int t = 0; t < 15; ++t) { WAITV(10); BAR(); ST(16 + t); load_stage(t + 3, slot); slot = g1next(slot); }
      WAITV(10); BAR(); ST(31);
      ST(2);
      g1_compute<true>(acc, w4, lane, bid, xa, wa, xb, wb);
      ST(3);
      const unsigned qo = WS_PQB + (hg * 64 + tile0) * 2048;
      f32x16 kv = f32x16{}, z = f32x16{};
      bf16x8 ones;
#pragma unroll
      for (int j = 0; j < 8; ++j) ones[j] = (__bf16)1.0f;
#pragma unroll
      for (int m = 0; m < 2; ++m) {
        f32x16 q, k;
#pragma unroll
        for (int r = 0; r < 16; ++r) { q[r] = phi(acc[m][0][r]); k[r] = phi(acc[m][1][r]); }
        st16(rws, l16, qo + m * 2048, pack8(q, 0)); st16(rws, l16, qo + m * 2048 + 1024, pack8(q, 1));
#pragma unroll
        for (int s = 0; s < 2; ++s) { const bf16x8 pk = pack8(k, s); kv = MFMA32(pk, pack8(acc[m][2], s), kv); z = MFMA32(pk, ones, z); }
        if (m == 0) { SBAR(); BAR(); SBAR(); }
      }
      const bool wm = w4 & 1;
      LAS f32x4* red = (LAS f32x4*)(uintptr_t)(L_BX + w4 * 4096);
#pragma unroll
      for (int g = 0; g < 2; ++g) {
        red[g * 64 + lane] = wm ? f32x4{kv[4 * g], kv[4 * g + 1], kv[4 * g + 2], kv[4 * g + 3]} : f32x4{kv[8 + 4 * g], kv[8 + 4 * g + 1], kv[8 + 4 * g + 2], kv[8 + 4 * g + 3]};
        red[(2 + g) * 64 + lane] = wm ? f32x4{z[4 * g], z[4 * g + 1], z[4 * g + 2], z[4 * g + 3]} : f32x4{z[8 + 4 * g], z[8 + 4 * g + 1], z[8 + 4 * g + 2], z[8 + 4 * g + 3]};
      }
#pragma unroll
      for (int j = 0; j < 8; ++j) { kvh[j] = wm ? kv[8 + j] : kv[j]; zh[j] = wm ? z[8 + j] : z[j]; }
      LBAR();
      { const LAS f32x4* rd = (const LAS f32x4*)(uintptr_t)(L_BX + (w4 ^ 1) * 4096);
#pragma unroll
        for (int g = 0; g < 2; ++g) { const f32x4 e = rd[g * 64 + lane], ez = rd[(2 + g) * 64 + lane];
#pragma unroll
          for (int j = 0; j < 4; ++j) { kvh[4 * g + j] += e[j]; zh[4 * g + j] += ez[j]; } }
        st16(rws, l16, WS_KVP + (hg * 16 + mt) * 2048 + (w4 & 1) * 1024, __builtin_convertvector(kvh, bf16x8));
        if (r32 == 0) st16(rws, hi * 16, WS_ZP + (hg * 16 + mt) * 64 + (w4 & 1) * 32, __builtin_convertvector(zh, bf16x8)); }
    }
  }
  ST(4);

  const unsigned gl = grp * 8192 + l16;
  const bf16x8 qa0 = ldsb128(L_Q + w4 * 4096 + l16), qa1 = ldsb128(L_Q + w4 * 4096 + 1024 + l16);
  const bf16x8 qb0 = ldsb128(L_Q + w4 * 4096 + 2048 + l16), qb1 = ldsb128(L_Q + w4 * 4096 + 3072 + l16);
  bf16x8 sumA;
  { const __bf16 v = (((lane >> 4) & 1) == ((lane >> 2) & 1)) ? (__bf16)1.0f : (__bf16)0.0f;
#pragma unroll
    for (int j = 0; j < 8; ++j) sumA[j] = v; }
  bf16x8 kf0 = ldsb128(kvslot(0) + gl), kf1 = ldsb128(kvslot(0) + gl + 1024), vf0, vf1 = bf16x8{}, p0, p1 = bf16x8{};
  f32x16 nma, nmb, sA, sB;
  { f32x16 s = MFMA32(kf0, qa0, f32x16{}); s = MFMA32(kf1, qa1, s);
    f32x16 u = MFMA32(kf0, qb0, f32x16{}); u = MFMA32(kf1, qb1, u);
    const float ma = rowmax16(s), mb = rowmax16(u);
#pragma unroll
    for (int r = 0; r < 16; ++r) { nma[r] = -ma; nmb[r] = -mb; sA[r] = s[r] - ma; } }
  f32x16 oa = f32x16{}, ob = f32x16{};
  f32x4 la = f32x4{}, lb = f32x4{};
  const unsigned vw2 = (wid * 8 + (lane >> 3)) * 2048 + n2 * 128 + (((lane & 7) ^ (((lane >> 4) & 1) << 2)) * 16);
  const int rot = (n2 & 7) * 2;
  auto load_w2 = [&](int t) { dma16(vw2, (t < 16 ? wob : woa) + (size_t)((t + rot) & 15) * 131072, (t & 15) * 8192 + wid * 1024); };
#define EX(X, i) X[i] = __builtin_amdgcn_exp2f(X[i])
#define HALF(SC, SN, Q0, Q1, NEG, OPREV, LPREV, OCUR, LCUR, RD1, RD2, RD3) do { \
    SN = MFMA32(kf0, Q0, NEG); SBAR(); RD1; \
    EX(SC, 0); EX(SC, 1); EX(SC, 2); EX(SC, 3); SBAR(); \
    OPREV = MFMA32(vf1, p1, OPREV); SBAR(); RD2; \
    EX(SC, 4); EX(SC, 5); EX(SC, 6); EX(SC, 7); SBAR(); \
    LPREV = MFMA16(sumA, p1, LPREV); SBAR(); \
    p0 = pack8(SC, 0); SBAR(); \
    SN = MFMA32(kf1, Q1, SN); SBAR(); RD3; \
    EX(SC, 8); EX(SC, 9); EX(SC, 10); EX(SC, 11); SBAR(); \
    OCUR = MFMA32(vf0, p0, OCUR); SBAR(); \
    EX(SC, 12); EX(SC, 13); EX(SC, 14); EX(SC, 15); SBAR(); \
    LCUR = MFMA16(sumA, p0, LCUR); SBAR(); \
    p1 = pack8(SC, 1); asm volatile("" : "+v"(p1)); SBAR(); \
  } while (0)
#define TILE(vaddr, kaddr) do { \
    HALF(sA, sB, qb0, qb1, nmb, ob, lb, oa, la, vf0 = ldsb128(vaddr), vf1 = ldsb128((vaddr) + 1024), { kf0 = ldsb128(kaddr); kf1 = ldsb128((kaddr) + 1024); }); \
    HALF(sB, sA, qa0, qa1, nma, oa, la, ob, lb, {}, {}, {}); \
  } while (0)
  u64 fl = 0;
#pragma unroll
  for (int stg = 0; stg < 16; ++stg) {
    const unsigned cur = kvslot(stg % 5) + gl;
    const int pslot = (stg + 4) % 5;
    const unsigned nxt = kvslot((stg + 1) % 5) + gl;
    TILE(cur + 2048, cur + 4096);
    if (stg < 15) {
      if (grp == 0) {
        if (stg == 7) WAITV(16); else if (stg == 8) WAITV(21); else if (stg == 9) WAITV(13); else if (stg < 11) WAITV(8); else if (stg == 11) WAITV(0); else if (stg == 14) WAITV(6);
      } else if (stg == 1 || stg == 6 || stg == 11) WAITV(0);
      if (stg == 14 && wid == 4) await_flags(ws, WS_FOB + ((m2 >> 1) * 32 + r32) * 8, fl, tag);
      LBAR();
      ST(32 + stg);
      if (grp == 0) {
        if (stg < 12) load_kv(stg + 4, pslot);
        if (stg >= 2 && stg < 6 && stg == 2 + (idx & 3) && w4 < 2) touch((w4 ? wob : woa) + (size_t)(bid * 64 + lane) * 128);
        if (stg == 6) { const char* p = ws + WS_KVP + (size_t)(h * 16 + w4 * 4) * 2048;
#pragma unroll
          for (int i = 0; i < 8; ++i) dma16(l16, p + i * 1024, L_KVP + w4 * 8192 + i * 1024); }
        if (stg == 7) { const char* p = ws + WS_PQB + (size_t)(h * 64 + qb * 8 + w4 * 2) * 2048;
#pragma unroll
          for (int i = 0; i < 4; ++i) dma16(l16, p + i * 1024, L_Q + w4 * 4096 + i * 1024);
          dma16(((r32 & 3) + 4 * (r32 >> 3)) * 64 + hi * 16, ws + WS_ZP + h * 1024 + (w4 & 1) * 32, L_Z + (w4 & 1) * 1024); }
      } else {
        if (wid == 4) {
          if (stg == 1 && lane == 0) post(ws, WS_FB + (nt * 16 + mt) * 8, tag);
          if (stg == 4) fl = peek(ws, WS_FB + ((h >> 1) * 16 + (lane & 15)) * 8);
          if (stg == 5) { await_flags(ws, WS_FB + ((h >> 1) * 16 + (lane & 15)) * 8, fl, tag); __builtin_amdgcn_fence(__ATOMIC_ACQUIRE, "agent"); }
          if (stg == 11 && lane == 0) post(ws, WS_FOB + (qb * 32 + h) * 8, tag);
          if (stg == 13) fl = peek(ws, WS_FOB + ((m2 >> 1) * 32 + r32) * 8);
        }
      }
      if (stg == 9) {
        float lo = 0.f, hi_ = 0.f;
#pragma unroll
        for (int p = 0; p < 16; ++p) { const unsigned v = *(const LAS unsigned*)(uintptr_t)(L_KVP + p * 2048 + wid * 256 + lane * 4); lo += __uint_as_float(v << 16); hi_ += __uint_as_float(v & 0xffff0000u); }
        *(LAS bf16x2*)(uintptr_t)(L_KVF + wid * 256 + lane * 4) = __builtin_convertvector(f32x2{lo, hi_}, bf16x2);
      }
      if (stg == 10) {
        const bf16x8 pq0 = ldsb128(L_Q + wid * 2048 + l16), pq1 = ldsb128(L_Q + wid * 2048 + 1024 + l16);
        f32x16 den = MFMA32(ldsb128(L_Z + l16), pq0, f32x16{}); den = MFMA32(ldsb128(L_Z + 1024 + l16), pq1, den);
        f32x16 num = MFMA32(ldsb128(L_KVF + l16), pq0, f32x16{}); num = MFMA32(ldsb128(L_KVF + 1024 + l16), pq1, num);
        const float d = ((((den[0] + den[1]) + (den[2] + den[3])) + ((den[4] + den[5]) + (den[6] + den[7]))) + (((den[8] + den[9]) + (den[10] + den[11])) + ((den[12] + den[13]) + (den[14] + den[15])))) + 1e-6f;
        num *= (1.0f / d);
        const unsigned oo = WS_O + (4u << 20) + ((qb * 8 + wid) * 64 + 2 * h) * 1024;
        st16(rws, l16, oo, pack8(num, 0)); st16(rws, l16, oo + 1024, pack8(num, 1));
      }
      if (stg >= 11 && stg < 14) { load_w2(2 * (stg - 11)); load_w2(2 * (stg - 11) + 1); }
      SBAR();
    }
    TILE(cur + 4096 + 2048, nxt);
  }
  ob = MFMA32(vf1, p1, ob); lb = MFMA16(sumA, p1, lb);
  ST(5);
  { LAS f32x4* ex = (LAS f32x4*)(uintptr_t)(L_AX + wid * 5120);
    const f32x16& so = grp ? oa : ob;
#pragma unroll
    for (int g = 0; g < 4; ++g) ex[g * 64 + lane] = f32x4{so[4 * g], so[4 * g + 1], so[4 * g + 2], so[4 * g + 3]};
    ex[256 + lane] = f32x4{grp ? la[0] : lb[0], grp ? nma[0] : nmb[0], 0.f, 0.f}; }
  const unsigned xo = WS_O + (m2 * 4 + w4) * 65536 + grp * 2048;
  bf16x8 xr[8][2];
#define LDX(i, base, t) do { const unsigned o_ = (base) + (((t) + rot) & 15) * 4096; xr[i][0] = ld16(rws, l16, o_); xr[i][1] = ld16(rws, l16 + 1024, o_); } while (0)
#pragma unroll
  for (int t = 0; t < 8; ++t) LDX(t, xo + (4u << 20), t);
  LBAR();
  ST(12);
#pragma unroll
  for (int t = 6; t < 10; ++t) load_w2(t);
  {
    f32x16 o = grp ? ob : oa;
    float l = grp ? lb[0] : la[0];
    const LAS f32x4* ex = (const LAS f32x4*)(uintptr_t)(L_AX + (wid ^ 4) * 5120);
    const f32x4 e = ex[256 + lane];
    const float n1 = grp ? nmb[0] : nma[0], n2_ = e[1], nm = __builtin_fminf(n1, n2_);
    const float f1 = __builtin_amdgcn_exp2f(nm - n1), f2 = __builtin_amdgcn_exp2f(nm - n2_);
    bool bad = !(l < 1.0e19f) || !(e[0] < 1.0e19f);
    l = l * f1 + e[0] * f2;
#pragma unroll
    for (int g = 0; g < 4; ++g) { const f32x4 x = ex[g * 64 + lane];
#pragma unroll
      for (int j = 0; j < 4; ++j) o[4 * g + j] = o[4 * g + j] * f1 + x[j] * f2; }
#pragma unroll
    for (int r = 0; r < 16; ++r) bad |= !(__builtin_fabsf(o[r]) < 3.0e38f);
    if (__builtin_expect(__any(bad), 0)) {
      const char* g = ws + WS_KVA + (size_t)h * 262144 + l16;
      const bf16x8 q0 = grp ? qb0 : qa0, q1 = grp ? qb1 : qa1;
      float m = -INFINITY; l = 0.f; o = f32x16{};
      for (int t = 0; t < 64; ++t) {
        const bf16x8 k0 = *(const bf16x8*)(g + t * 4096), k1 = *(const bf16x8*)(g + t * 4096 + 1024);
        const bf16x8 v0 = *(const bf16x8*)(g + t * 4096 + 2048), v1 = *(const bf16x8*)(g + t * 4096 + 3072);
        f32x16 s = MFMA32(k0, q0, f32x16{}); s = MFMA32(k1, q1, s);
        const float mn = __builtin_fmaxf(m, rowmax16(s));
        const float al = __builtin_amdgcn_exp2f(m - mn);
        m = mn;
#pragma unroll
        for (int r = 0; r < 16; ++r) s[r] = __builtin_amdgcn_exp2f(s[r] - mn);
        const bf16x8 a0 = pack8(s, 0), a1 = pack8(s, 1);
        f32x4 ls = MFMA16(sumA, a0, f32x4{}); ls = MFMA16(sumA, a1, ls);
        o *= al; l = l * al + ls[0];
        o = MFMA32(v0, a0, o); o = MFMA32(v1, a1, o);
      }
    }
    o *= (1.0f / l);
    const unsigned oo = WS_O + ((qb * 8 + w4 * 2 + grp) * 64 + 2 * h) * 1024;
    st16(rws, l16, oo, pack8(o, 0)); st16(rws, l16, oo + 1024, pack8(o, 1));
  }
  ST(13);
  asm volatile("s_waitcnt vmcnt(0) lgkmcnt(0)\n\ts_barrier" ::: "memory");
  if (tid == 0) post(ws, WS_FOA + (qb * 32 + h) * 8, tag);
#pragma unroll
  for (int t = 10; t < 16; ++t) load_w2(t);
  ST(6);

  unsigned wr0, wr1;
  { const int g1 = (lane >> 4) & 1, q = (lane & 15) >> 2, p = lane & 3;
    const int ncol = 16 * g1 + 8 * (p & 1) + 4 * (p >> 1);
    const unsigned sw = ((q >> 1) & 1) << 2;
    const unsigned rowb = (grp * 32 + 4 * hi + q) * 128 + ((ncol >> 2) & 1) * 8;
    wr0 = rowb + (((ncol >> 3)) ^ sw) * 16; wr1 = rowb + (((ncol >> 3) + 4) ^ sw) * 16; }
  f32x16 c0, c1;
#define G2STEP(t, XR) do { const unsigned so = (t) * 8192; \
    const bf16x8 w00 = ldstr2(so + wr0, so + wr0 + 1024), w01 = ldstr2(so + wr1, so + wr1 + 1024); \
    const bf16x8 w10 = ldstr2(so + wr0 + 2048, so + wr0 + 3072), w11 = ldstr2(so + wr1 + 2048, so + wr1 + 3072); \
    c0 = MFMA32(w00, XR[0], c0); c1 = MFMA32(w01, XR[0], c1); c0 = MFMA32(w10, XR[1], c0); c1 = MFMA32(w11, XR[1], c1); } while (0)
  auto g2_finish = [&](int prob) {
    LAS f32x4* ex = (LAS f32x4*)(uintptr_t)(L_G2X + wid * 4096);
    const f32x16& so = grp ? c0 : c1;
#pragma unroll
    for (int g = 0; g < 4; ++g) ex[g * 64 + lane] = f32x4{so[4 * g], so[4 * g + 1], so[4 * g + 2], so[4 * g + 3]};
    LBAR();
    const LAS f32x4* rx = (const LAS f32x4*)(uintptr_t)(L_G2X + (wid ^ 4) * 4096);
    f32x16 a = grp ? c1 : c0;
#pragma unroll
    for (int g = 0; g < 4; ++g) { const f32x4 e = rx[g * 64 + lane]; a[4 * g] += e[0]; a[4 * g + 1] += e[1]; a[4 * g + 2] += e[2]; a[4 * g + 3] += e[3]; }
    u16* op = out + (size_t)(m2 * 128 + w4 * 32 + r32) * 2048 + prob * 1024 + n2 * 64 + grp * 32 + hi * 8;
    *(bf16x8*)op = pack8(a, 0); *(bf16x8*)(op + 16) = pack8(a, 1);
  };
  c0 = f32x16{}; c1 = f32x16{};
#pragma unroll
  for (int t = 0; t < 8; ++t) {
    G2STEP(t, xr[t]);
    LDX(t, xo + (4u << 20), t + 8);
  }
  asm volatile("s_waitcnt vmcnt(16) lgkmcnt(0)\n\ts_barrier" ::: "memory");
  ST(14);
#pragma unroll
  for (int t = 16; t < 24; ++t) load_w2(t);
  if (wid == 4) fl = peek(ws, WS_FOA + ((m2 >> 1) * 32 + r32) * 8);
#pragma unroll
  for (int t = 8; t < 16; ++t) G2STEP(t, xr[t - 8]);
  ST(15);
  if (wid == 4) await_flags(ws, WS_FOA + ((m2 >> 1) * 32 + r32) * 8, fl, tag);
  LBAR();
  ST(7);
#pragma unroll
  for (int t = 0; t < 8; ++t) LDX(t, xo, t);
#pragma unroll
  for (int t = 24; t < 32; ++t) load_w2(t);
  if (bid == 0 && tid == 256) post(ws, WS_EPOCH, tag);
  WAITV(24);
  g2_finish(1);
  ST(8);
  c0 = f32x16{}; c1 = f32x16{};
#pragma unroll
  for (int t = 0; t < 8; ++t) {
    G2STEP(t, xr[t]);
    LDX(t, xo, t + 8);
  }
  asm volatile("s_waitcnt vmcnt(16) lgkmcnt(0)\n\ts_barrier" ::: "memory");
#pragma unroll
  for (int t = 8; t < 16; ++t) G2STEP(t, xr[t - 8]);
  ST(9);
  g2_finish(0);
}

extern "C" void kernel_launch(void* const* d_in, const int* in_sizes, int n_in, void* d_out, int out_size, void* d_ws, size_t ws_size, hipStream_t stream) {
  (void)hipFuncSetAttribute((const void*)k_fused, hipFuncAttributeMaxDynamicSharedMemorySize, LDS_BYTES);
  k_fused<<<256, 512, LDS_BYTES, stream>>>((const char*)d_in[0], (const char*)d_in[1], (const char*)d_in[2], (const char*)d_in[3], (const char*)d_in[4], (const char*)d_in[5],
                                           (u16*)d_out, (char*)d_ws);
}
```

```cpp
#include <hip/hip_runtime.h>
#include <hip/hip_bf16.h>

typedef unsigned short u16;
typedef unsigned long long u64;
typedef __bf16 bf16x8 __attribute__((ext_vector_type(8)));
typedef __bf16 bf16x2 __attribute__((ext_vector_type(2)));
typedef float f32x2 __attribute__((ext_vector_type(2)));
typedef float f32x8 __attribute__((ext_vector_type(8)));
typedef float f32x4 __attribute__((ext_vector_type(4)));
typedef float f32x16 __attribute__((ext_vector_type(16)));
typedef short s16x4 __attribute__((ext_vector_type(4)));
typedef short s16x8 __attribute__((ext_vector_type(8)));
typedef unsigned u32x4 __attribute__((ext_vector_type(4)));
#define LAS __attribute__((address_space(3)))
#define GAS __attribute__((address_space(1)))
#define DI __device__ __forceinline__
#define MFMA32(a, b, c) __builtin_amdgcn_mfma_f32_32x32x16_bf16((a), (b), (c), 0, 0, 0)
#define MFMA16(a, b, c) __builtin_amdgcn_mfma_f32_16x16x32_bf16((a), (b), (c), 0, 0, 0)
#define SBAR() __builtin_amdgcn_sched_barrier(0)
#define RLX_AGENT __ATOMIC_RELAXED, __HIP_MEMORY_SCOPE_AGENT
#define WAITV(n) asm volatile("s_waitcnt vmcnt(" #n ")" ::: "memory")
#define BAR() asm volatile("s_barrier" ::: "memory")
#define LBAR() do { asm volatile("" ::: "memory"); __builtin_amdgcn_s_waitcnt(0xC07F); asm volatile("s_barrier" ::: "memory"); } while (0)

constexpr unsigned WS_EPOCH = 0;
constexpr unsigned WS_FA = 4096;
constexpr unsigned WS_FB = WS_FA + 2048;
constexpr unsigned WS_FOB = WS_FB + 2048;
constexpr unsigned WS_FOA = WS_FOB + 2048;
constexpr unsigned WS_QA = 1u << 20;
constexpr unsigned WS_KVA = WS_QA + (4u << 20);
constexpr unsigned WS_PQB = WS_KVA + (8u << 20);
constexpr unsigned WS_KVP = WS_PQB + (4u << 20);
constexpr unsigned WS_ZP = WS_KVP + (1u << 20);
constexpr unsigned WS_O = WS_ZP + (64u << 10);
constexpr unsigned WS_END = WS_O + (8u << 20);

constexpr unsigned G1_STAGE = 40960, G1_WOFF = 16384;
constexpr unsigned L_Q = 122880;
constexpr unsigned L_MISC = 155648;
constexpr unsigned L_DUMMY = L_MISC, L_Z = L_MISC + 1024, L_KVF = L_MISC + 3072;
constexpr unsigned L_BX = 0;
constexpr unsigned L_KVP = 0;
constexpr unsigned L_LM = L_MISC + 1024;
constexpr unsigned LDS_BYTES = 163840;
constexpr unsigned g1slot(int s) { return s == 1 ? 0u : (s == 0 ? 40960u : 81920u); }
DI unsigned g1next(unsigned a) { return a == 0u ? 81920u : a - 40960u; }
constexpr unsigned kvslot(int s) { return s == 0 ? 139264u : (s == 1 ? 106496u : (s == 2 ? 90112u : 65536u)); }
constexpr unsigned w2slot(int t) {
  return t < 8 ? t * 8192u : t == 8 ? 81920u : t < 11 ? L_Q + (t - 9) * 8192u : t < 19 ? (t - 11) * 8192u
       : t < 27 ? w2slot(t - 11)
       : t < 29 ? kvslot(0) + (t - 27) * 8192u : t < 31 ? kvslot(1) + (t - 29) * 8192u : L_MISC;
}

constexpr int SL(int stg, int i) { return stg * 8 + i; }
constexpr int kv_at(int sl) { return ((sl >> 3) <= 12 && ((sl & 7) == 4 || (sl & 7) == 6)) ? 1 : 0; }
constexpr int lin_at(int sl) { return sl == SL(3, 5) ? 0 : sl == SL(3, 7) ? 1 : (sl >= SL(4, 0) && sl <= SL(4, 3)) ? sl - SL(4, 0) + 2 : -1; }
constexpr int w_at(int sl) {
  return sl == SL(8, 5) ? 0 : sl == SL(8, 7) ? 1 : (sl >= SL(9, 0) && sl <= SL(9, 3)) ? sl - SL(9, 0) + 2 : sl == SL(9, 5) ? 6 : sl == SL(9, 7) ? 7
       : (sl >= SL(10, 0) && sl <= SL(10, 2)) ? sl - SL(10, 0) + 8 : sl == SL(12, 4) ? 11 : sl == SL(12, 5) ? 12 : sl == SL(12, 7) ? 13 : sl == SL(13, 0) ? 14
       : (sl >= SL(13, 4) && sl <= SL(13, 7)) ? sl - SL(13, 4) + 15 : (sl >= SL(14, 4) && sl <= SL(14, 7)) ? sl - SL(14, 4) + 19 : -1;
}
constexpr int H0 = SL(11, 4) / 2;
constexpr int x_slot(int k) { return k < 8 ? SL(10, 4) + k : 2 * (H0 + ((k - 8) >> 1) + 1) + (k & 1) * 2; }
constexpr int x_cnt(int sl) { int n = 0; for (int k = 0; k < 32; ++k) n += x_slot(k) == sl; return n; }
constexpr int nops(int sl) { return kv_at(sl) + (lin_at(sl) >= 0 ? (lin_at(sl) == 5 ? 2 : 1) : 0) + (sl == SL(7, 1) ? 2 : 0) + (w_at(sl) >= 0 ? 1 : 0) + x_cnt(sl); }
constexpr int ops_between(int a, int b) { int n = 0; for (int i = a; i < b; ++i) n += nops(i); return n; }
constexpr int imin(int a, int b) { return a < b ? a : b; }
template <int N> DI void waitv() { asm volatile("s_waitcnt vmcnt(%0)" ::"n"(N) : "memory"); }

template <int N> struct IC { static constexpr int value = N; };
DI bf16x8 pack8(const f32x16& x, int s) {
  f32x8 v = {x[8 * s], x[8 * s + 1], x[8 * s + 2], x[8 * s + 3], x[8 * s + 4], x[8 * s + 5], x[8 * s + 6], x[8 * s + 7]};
  return __builtin_convertvector(v, bf16x8);
}
DI void dma16(unsigned voff, const void* sbase, unsigned ldsaddr) {
  asm volatile("s_mov_b32 m0, %2\n\ts_nop 0\n\tglobal_load_lds_dwordx4 %0, %1" ::"v"(voff), "s"(sbase), "s"(__builtin_amdgcn_readfirstlane(ldsaddr)) : "memory");
}
DI void dma16c(unsigned voff, const void* sbase, unsigned ldsaddr) {
  asm volatile("s_mov_b32 m0, %2\n\ts_nop 0\n\tglobal_load_lds_dwordx4 %0, %1 sc1" ::"v"(voff), "s"(sbase), "s"(__builtin_amdgcn_readfirstlane(ldsaddr)) : "memory");
}
DI void touch(const char* p) {
  asm volatile("s_mov_b32 m0, %1\n\ts_nop 0\n\tglobal_load_lds_dword %0, off" ::"v"(p), "s"(L_DUMMY) : "memory");
}
DI bf16x8 ldsb128(unsigned addr) { return *(const LAS bf16x8*)(uintptr_t)(addr); }
DI bf16x8 ldstr2(unsigned a0, unsigned a1) {
  s16x4 lo = __builtin_amdgcn_ds_read_tr16_b64_v4i16((LAS s16x4*)(uintptr_t)(a0));
  s16x4 hi = __builtin_amdgcn_ds_read_tr16_b64_v4i16((LAS s16x4*)(uintptr_t)(a1));
  s16x8 t = __builtin_shufflevector(lo, hi, 0, 1, 2, 3, 4, 5, 6, 7);
  return __builtin_bit_cast(bf16x8, t);
}
DI float phi(float t) {
  const float e = __builtin_amdgcn_fmed3f(__builtin_amdgcn_exp2f(t * 1.4426950408889634f), 0.f, 1.f);
  return __builtin_amdgcn_fmed3f(t, 0.f, 3.4028235e38f) + (e + 1e-6f);
}
typedef __amdgpu_buffer_rsrc_t rsrc_t;
DI void st16(rsrc_t r, unsigned voff, unsigned soff, const bf16x8& v) {
  __builtin_amdgcn_raw_buffer_store_b128(__builtin_bit_cast(u32x4, v), r, voff + soff, 0, 16);
}
DI bf16x8 ld16(rsrc_t r, unsigned voff, unsigned soff) {
  return __builtin_bit_cast(bf16x8, __builtin_amdgcn_raw_buffer_load_b128(r, voff, soff, 16));
}
template <int OFF> DI void ldx_async(bf16x8& dst, unsigned voff, const char* sbase) {
  asm volatile("global_load_dwordx4 %0, %1, %2 offset:%3 sc1" : "=v"(dst) : "v"(voff), "s"(sbase), "n"(OFF) : "memory");
}
template <int N> DI void ldx_wait(bf16x8& dst) { asm volatile("s_waitcnt vmcnt(%1)" : "+v"(dst) : "n"(N) : "memory"); }
DI void peek_async(u64& dst, unsigned voff, const char* sbase) {
  asm volatile("global_load_dwordx2 %0, %1, %2 sc1" : "=v"(dst) : "v"(voff), "s"(sbase) : "memory");
}
template <int N> DI void peek_wait(u64& dst) { asm volatile("s_waitcnt vmcnt(%1)" : "+v"(dst) : "n"(N) : "memory"); }
DI void post(char* ws, unsigned off, u64 v) { __hip_atomic_store((GAS u64*)(ws + off), v, RLX_AGENT); }
DI u64 peek(char* ws, unsigned off) { return __hip_atomic_load((GAS u64*)(ws + off), RLX_AGENT); }
DI void await_flags(char* ws, unsigned off, u64 first, u64 tag) {
  if (__builtin_expect(__all(first == tag), 1)) return;
  for (unsigned spins = 0; spins < (1u << 18); ++spins) { if (__all(peek(ws, off) == tag)) break; __builtin_amdgcn_s_sleep(2); }
}
DI float rowmax16(const f32x16& p) {
  float a = __builtin_fmaxf(__builtin_fmaxf(p[0], p[1]), p[2]);
#pragma unroll
  for (int r = 3; r < 15; r += 2) a = __builtin_fmaxf(__builtin_fmaxf(a, p[r]), p[r + 1]);
  a = __builtin_fmaxf(a, p[15]);
  auto rr = __builtin_amdgcn_permlane32_swap(__float_as_uint(a), __float_as_uint(a), false, false);
  return __builtin_fmaxf(__uint_as_float(rr[0]), __uint_as_float(rr[1]));
}

DI void g1_touch(const char* bx, const char* bw, int hf, int w4, int bid, int lane) {
  if (w4 < 3) {
    const unsigned off = w4 == 0 ? (unsigned)((lane >> 3) * 2048 + (lane & 7) * 128) : (unsigned)((lane < 48 ? lane : lane - 48) * 128);
    const char* base = w4 == 0 ? bx + (size_t)bid * 16384 + hf * 1024 : bw + (size_t)(hf * 512 + (w4 - 1) * 256 + bid) * 6144;
    asm volatile("s_mov_b32 m0, %2\n\ts_nop 0\n\tglobal_load_lds_dword %0, %1" ::"v"(off), "s"(base), "s"(L_DUMMY) : "memory");
  }
}

DI bf16x8 pack44(const f32x4& a, const f32x4& b) { return __builtin_convertvector(__builtin_shufflevector(a, b, 0, 1, 2, 3, 4, 5, 6, 7), bf16x8); }
template <bool ISB>
DI void g1_compute(f32x4 (&acc)[4][6], int w4, int lane) {
  const int wm = w4 & 1, wh = w4 >> 1, g = lane >> 4, pp = (lane & 15) >> 2, c4 = lane & 3;
  unsigned xa0, wa0, wa1;
  { const int row = 16 * (pp >> 1) + 4 * (pp & 1) + c4;
    const unsigned f = ((row >> 1) & 3) | ((pp >> 1) << 2);
    xa0 = (wm * 64 + row) * 128 + ((g ^ f) * 16);
    const unsigned sw = (((pp >> 1) & 1) << 2) | (g & 1);
    const unsigned rb = G1_WOFF + (8 * g + pp) * 128 + (c4 & 1) * 8;
    wa0 = rb + ((wh * 4 + 2 * (c4 >> 1)) ^ sw) * 16; wa1 = rb + ((wh * 4 + 2 * (c4 >> 1) + 1) ^ sw) * 16; }
  bf16x8 fx[2][4];
  s16x4 wl[2][6], wu[2][6];
#define TRD(a) __builtin_amdgcn_ds_read_tr16_b64_v4i16((LAS s16x4*)(uintptr_t)(a))
#define RDX(b, i, XA) do { fx[b][i] = ldsb128((XA) + ((i) & 1) * 1024 + ((i) >> 1) * 4096); SBAR(); } while (0)
#define RWL(b, n, s2) do { wl[b][n] = TRD((((n) & 1) ? w1 : w0) + ((n) >> 1) * 8192 + (s2) * 4096); SBAR(); } while (0)
#define RWU(b, n, s2) do { wu[b][n] = TRD((((n) & 1) ? w1 : w0) + ((n) >> 1) * 8192 + (s2) * 4096 + 512); SBAR(); } while (0)
#define FW(b, n) __builtin_bit_cast(bf16x8, __builtin_shufflevector(wl[b][n], wu[b][n], 0, 1, 2, 3, 4, 5, 6, 7))
#define MM(b, i, n) do { if ((n) < 2 || ((n) < 4 && !ISB)) acc[i][n] = MFMA16(FW(b, n), fx[b][i], acc[i][n]); else acc[i][n] = MFMA16(fx[b][i], FW(b, n), acc[i][n]); SBAR(); } while (0)
#define SUB(b, XA, s2, HEAD, TAIL) do { constexpr int o = (b) ^ 1; \
    MM(b, 0, 0); HEAD; SBAR(); MM(b, 0, 1); RDX(o, 0, XA); MM(b, 0, 2); RWL(o, 0, s2); MM(b, 0, 3); RWU(o, 0, s2); MM(b, 0, 4); RWL(o, 1, s2); MM(b, 0, 5); RWU(o, 1, s2); \
    MM(b, 1, 0); RWL(o, 2, s2); MM(b, 1, 1); RWU(o, 2, s2); MM(b, 1, 2); RWL(o, 3, s2); MM(b, 1, 3); RWU(o, 3, s2); MM(b, 1, 4); RWL(o, 4, s2); MM(b, 1, 5); RWU(o, 4, s2); \
    MM(b, 2, 0); RWL(o, 5, s2); MM(b, 2, 1); RWU(o, 5, s2); MM(b, 2, 2); RDX(o, 1, XA); MM(b, 2, 3); RDX(o, 2, XA); MM(b, 2, 4); RDX(o, 3, XA); MM(b, 2, 5); \
    MM(b, 3, 0); TAIL; SBAR(); MM(b, 3, 1); MM(b, 3, 2); MM(b, 3, 3); MM(b, 3, 4); MM(b, 3, 5); } while (0)
  unsigned slot = g1slot(ISB ? 1 : 0);
  unsigned x0 = slot + xa0, x1 = x0 ^ 64, w0 = slot + wa0, w1 = slot + wa1;
#pragma unroll
  for (int i = 0; i < 4; ++i) RDX(0, i, x0);
#pragma unroll
  for (int n = 0; n < 6; ++n) { RWL(0, n, 0); RWU(0, n, 0); }
  __builtin_amdgcn_s_setprio(3);
#pragma unroll 1
  for (int t = 0; t < 16; ++t) {
    SUB(0, x1, 1, {}, { slot = g1next(slot); x0 = slot + xa0; w0 = slot + wa0; asm volatile("" : "+v"(x0), "+v"(w0)); });
    w1 = slot + wa1; x1 = x0 ^ 64;
    SUB(1, x0, 0, { if (ISB && t == 0) WAITV(0); LBAR(); }, {});
  }
  if (!ISB) __builtin_amdgcn_s_setprio(0);
#undef RDX
#undef RWL
#undef RWU
#undef FW
#undef MM
#undef SUB
}

__global__ __launch_bounds__(512, 2) void k_fused(const char* __restrict__ xa, const char* __restrict__ wa, const char* __restrict__ woa,
                                                   const char* __restrict__ xb, const char* __restrict__ wb, const char* __restrict__ wob,
                                                   u16* __restrict__ out, char* __restrict__ ws) {
  extern __shared__ __attribute__((aligned(16))) char lds[];
  const int bid = blockIdx.x, xcd = bid & 7, idx = bid >> 3;
  const int tid = threadIdx.x, lane = tid & 63;
  const int wid = __builtin_amdgcn_readfirstlane(tid >> 6);
  const int w4 = wid & 3, grp = wid >> 2, hi = lane >> 5, r32 = lane & 31;
  const unsigned l16 = lane * 16;
  const int mt = (xcd >> 2) * 8 + (idx & 7), nt = (xcd & 3) * 4 + (idx >> 3);
  const int h = xcd * 4 + (idx >> 3), qb = idx & 7;
  const int m2 = xcd * 2 + (idx & 1), n2 = idx >> 1;
  const rsrc_t rws = __builtin_amdgcn_make_buffer_rsrc(ws, 0, WS_END, 0x00020000);
  u64 tag;
#define TAKE_TAG(ev) tag = (((u64)__builtin_amdgcn_readfirstlane((unsigned)((ev) >> 32)) << 32) | (unsigned)__builtin_amdgcn_readfirstlane((unsigned)(ev))) + 1

  const int ntq = (nt & 12) | ((nt + 1) & 3), ntv = (nt & 12) | ((nt + 2) & 3);
  unsigned vx[2], vw[6];
#pragma unroll
  for (int i = 0; i < 2; ++i) vx[i] = (mt * 128 + w4 * 32 + i * 16 + (lane >> 3)) * 2048 + (((lane & 7) ^ (4 * i + (lane >> 4))) * 16);
#pragma unroll
  for (int i = 0; i < 6; ++i) { const int u = w4 * 6 + i; vw[i] = ((u & 7) * 8 + (lane >> 3)) * 6144 + (u >> 3) * 2048 + ((u >> 3) == 0 ? ntq : nt) * 128 + (((lane & 7) ^ (((lane >> 4) & 1) << 2) ^ (u & 1)) * 16); }
  auto load_stage = [&](int gt, unsigned l) {
    const char* bx = (gt < 16 ? xa : xb) + (gt & 15) * 128;
    const char* bw = (gt < 16 ? wa : wb) + (size_t)(gt & 15) * 64 * 6144;
    dma16(vx[0], bx, l + w4 * 4096); dma16(vx[0], bx + 16384, l + w4 * 4096 + 1024);
    dma16(vx[1], bx, l + w4 * 4096 + 2048); dma16(vx[1], bx + 16384, l + w4 * 4096 + 3072);
#pragma unroll
    for (int i = 0; i < 6; ++i) dma16(vw[i], (gt < 16 && w4 * 6 + i >= 16) ? bw + (ntv - nt) * 128 : bw, l + G1_WOFF + (w4 * 6 + i) * 1024);
  };
  const char* kvsrc = ws + WS_KVA + (size_t)h * 262144 + (w4 >> 1) * 131072 + (w4 & 1) * 4096;
  auto load_kv = [&](int stg, int slot) {
    const char* p = kvsrc + stg * 8192; const unsigned l = kvslot(slot) + w4 * 4096;
#pragma unroll
    for (int i = 0; i < 4; ++i) dma16c(l16, p + i * 1024, l + i * 1024);
  };

  {
    f32x4 acc[4][6];
    const unsigned fo = (lane >> 5) * 1024 + ((lane >> 4) & 1) * 512 + (16 * ((lane >> 3) & 1) + (lane & 7)) * 16;
    const int hg = nt * 2 + (w4 >> 1), tile0 = mt * 4 + (w4 & 1) * 2;
    auto zero_acc = [&]() __attribute__((always_inline)) {
#pragma unroll
      for (int i = 0; i < 4; ++i)
#pragma unroll
        for (int n = 0; n < 6; ++n) { acc[i][n] = f32x4{}; asm volatile("" : "+v"(acc[i][n])); } };
    if (grp == 0) {
      g1_touch(xa, wa, 0, w4, bid, lane);
      zero_acc();
      BAR();
      g1_compute<false>(acc, w4, lane);
      constexpr float QS = 0.17677669529663687f * 1.4426950408889634f;
      const unsigned qo = WS_QA + ((ntq * 2 + (w4 >> 1)) * 64 + tile0) * 2048, ko = WS_KVA + (hg * 64 + tile0) * 4096, vo = WS_KVA + ((ntv * 2 + (w4 >> 1)) * 64 + tile0) * 4096;
      load_stage(18, g1slot(0));
      auto stq = [&](int j) __attribute__((always_inline)) {
        if (j < 4) st16(rws, fo, ko + (j >> 1) * 4096 + (j & 1) * 128, pack44(acc[j][2], acc[j][3]));
        else if (j < 8) st16(rws, fo, vo + ((j >> 1) & 1) * 4096 + 2048 + (j & 1) * 128, pack44(acc[j & 2][4 + (j & 1)], acc[(j & 2) + 1][4 + (j & 1)]));
        else st16(rws, fo, qo + ((j >> 1) & 1) * 2048 + (j & 1) * 128, pack44(acc[j & 3][0] * QS, acc[j & 3][1] * QS)); };
#pragma unroll
      for (int t = 16; t < 25; ++t) {
        if (t > 16) WAITV(12);
        BAR(); load_stage(t + 3, g1slot((t + 3) % 3));
        if (t < 19) { stq(2 * (t - 16)); stq(2 * (t - 16) + 1); } else stq(t - 13);
        SBAR();
      }
      u64 ev;
      peek_async(ev, 0u, ws + WS_EPOCH);
      WAITV(12); BAR();
      peek_wait<0>(ev); TAKE_TAG(ev);
      load_stage(28, g1slot(1));
      BAR();
      if (tid == 0) post(ws, WS_FA + (nt * 16 + mt) * 8, tag);
      load_stage(29, g1slot(2));
      WAITV(10); BAR(); load_stage(30, g1slot(0));
      WAITV(10); BAR(); load_stage(31, g1slot(1));
      WAITV(10); BAR();
      if (wid == 0) await_flags(ws, WS_FA + ((h >> 3) * 64 + lane) * 8, peek(ws, WS_FA + ((h >> 3) * 64 + lane) * 8), tag);
      WAITV(0); BAR();
      { const char* qsrc = ws + WS_QA + (size_t)(h * 64 + qb * 8 + w4 * 2) * 2048;
#pragma unroll
        for (int i = 0; i < 4; ++i) dma16c(l16, qsrc + i * 1024, L_Q + w4 * 4096 + i * 1024); }
      load_kv(0, 0);
      BAR();
      load_kv(1, 1); load_kv(2, 2);
      WAITV(8); BAR();
    } else {
      u64 ev;
      peek_async(ev, 0u, ws + WS_EPOCH);
      load_stage(0, g1slot(0)); load_stage(1, g1slot(1)); load_stage(2, g1slot(2));
      zero_acc();
      peek_wait<20>(ev); TAKE_TAG(ev);
      BAR();
      unsigned slot = g1slot(0);
#pragma unroll 1
      for (int t = 0; t < 15; ++t) { WAITV(10); BAR(); load_stage(t + 3, slot); slot = g1next(slot);
        const int u = t - (idx & 1); if ((u & 1) == 0 && u >= 0 && u < 6) { const int e = u >> 1; g1_touch(e ? xb : xa, e ? wb : wa, (e + 1) & 1, w4, bid, lane); } }
      WAITV(10); BAR();
      g1_compute<true>(acc, w4, lane);
      const unsigned qo = WS_PQB + ((ntq * 2 + (w4 >> 1)) * 64 + tile0) * 2048;
      f32x4 kv[2][2] = {}, z[2] = {};
      bf16x8 ones;
#pragma unroll
      for (int j = 0; j < 8; ++j) ones[j] = (__bf16)1.0f;
#pragma unroll
      for (int m = 0; m < 2; ++m) {
#pragma unroll
        for (int i = 2 * m; i < 2 * m + 2; ++i)
#pragma unroll
          for (int n = 2; n < 4; ++n)
#pragma unroll
            for (int r = 0; r < 4; ++r) acc[i][n][r] = phi(acc[i][n][r]);
        const bf16x8 v0 = pack44(acc[2 * m][4], acc[2 * m + 1][4]), v1 = pack44(acc[2 * m][5], acc[2 * m + 1][5]);
#pragma unroll
        for (int cd = 0; cd < 2; ++cd) { const bf16x8 pk = pack44(acc[2 * m][2 + cd], acc[2 * m + 1][2 + cd]);
          kv[cd][0] = MFMA16(pk, v0, kv[cd][0]); kv[cd][1] = MFMA16(pk, v1, kv[cd][1]); z[cd] = MFMA16(pk, ones, z[cd]); }
      }
      const bool wm = w4 & 1;
      LAS f32x4* red = (LAS f32x4*)(uintptr_t)(L_BX + w4 * 4096);
      red[lane] = wm ? kv[0][0] : kv[0][1]; red[64 + lane] = wm ? kv[1][0] : kv[1][1]; red[128 + lane] = z[0]; red[192 + lane] = z[1];
      SBAR(); LBAR(); SBAR();
      const LAS f32x4* rd = (const LAS f32x4*)(uintptr_t)(L_BX + (w4 ^ 1) * 4096);
      const f32x4 e0 = rd[lane], e1 = rd[64 + lane], e2 = rd[128 + lane], e3 = rd[192 + lane];
#pragma unroll
      for (int i = 0; i < 4; ++i) {
#pragma unroll
        for (int n = 0; n < 2; ++n)
#pragma unroll
          for (int r = 0; r < 4; ++r) acc[i][n][r] = phi(acc[i][n][r]);
        st16(rws, fo, qo + (i >> 1) * 2048 + (i & 1) * 128, pack44(acc[i][0], acc[i][1]));
        if (i == 0) {
          st16(rws, fo, WS_KVP + (hg * 16 + mt) * 2048 + (w4 & 1) * 128, pack44((wm ? kv[0][1] : kv[0][0]) + e0, (wm ? kv[1][1] : kv[1][0]) + e1));
          if ((lane & 15) == 0 && !wm) st16(rws, (lane >> 4) * 16, WS_ZP + (hg * 16 + mt) * 64, pack44(z[0] + e2, z[1] + e3));
        }
      }
    }
  }

  unsigned gl1 = grp * 8192 + l16 + 65536, gl2 = gl1 + 65536;
  asm volatile("" : "+v"(gl1), "+v"(gl2));
#define KVA(s) ((kvslot(s) >= 131072 ? gl2 - 131072 : gl1 - 65536) + kvslot(s))
  const unsigned qal = L_Q + w4 * 4096 + grp * 2048 + l16, qbl = qal ^ 2048;
  const bf16x8 qa0 = ldsb128(qal), qa1 = ldsb128(qal + 1024), qb0 = ldsb128(qbl), qb1 = ldsb128(qbl + 1024);
  bf16x8 sumA;
  { const __bf16 v = (((lane >> 4) & 1) == ((lane >> 2) & 1)) ? (__bf16)1.0f : (__bf16)0.0f;
#pragma unroll
    for (int j = 0; j < 8; ++j) sumA[j] = v; }
  bf16x8 kf0 = ldsb128(KVA(0)), kf1 = ldsb128(KVA(0) + 1024), vf0, vf1 = bf16x8{}, p0, p1 = bf16x8{};
  f32x16 nm, sA, sB;
  { f32x16 s = MFMA32(kf0, qa0, f32x16{}); s = MFMA32(kf1, qa1, s);
    f32x16 u = MFMA32(kf0, qb0, f32x16{}); u = MFMA32(kf1, qb1, u);
#pragma unroll
    for (int r = 0; r < 16; ++r) u[r] = __builtin_fmaxf(u[r], s[r]);
    const float m = rowmax16(u);
#pragma unroll
    for (int r = 0; r < 16; ++r) { nm[r] = -m; sA[r] = s[r] - m; } }
  f32x16 oa = f32x16{}, ob = f32x16{};
  f32x4 la = f32x4{}, lb = f32x4{};
  const unsigned vw2 = (wid * 8 + (lane >> 3)) * 2048 + n2 * 128 + (((lane & 7) ^ (((lane >> 4) & 1) << 2)) * 16);
  auto load_w2 = [&](int t) __attribute__((always_inline)) { dma16(vw2, (t < 16 ? wob : woa) + (size_t)(t & 15) * 131072, w2slot(t) + wid * 1024); };
  const unsigned xo = WS_O + (m2 * 4 + w4) * 65536 + grp * 2048;
  const char* xb2 = ws + xo + (4u << 20);
  unsigned wr0, wr1;
  { const int g1 = (lane >> 4) & 1, q = (lane & 15) >> 2, p = lane & 3;
    const int ncol = 16 * g1 + 8 * (p & 1) + 4 * (p >> 1);
    const unsigned sw = ((q >> 1) & 1) << 2;
    const unsigned rowb = (grp * 32 + 4 * hi + q) * 128 + ((ncol >> 2) & 1) * 8;
    wr0 = rowb + (((ncol >> 3) + 4 * grp) ^ sw) * 16; wr1 = wr0 ^ 64; }
  unsigned wh0 = wr0 + 102400, wh1 = wr1 + 102400, wm0 = wr0 + 51200, wm1 = wr1 + 51200;
  asm volatile("" : "+v"(wh0), "+v"(wh1));
#define WRA(n, a) ((a) >= 102400 ? ((n) ? wh1 : wh0) + ((a) - 102400) : (a) >= 61440 ? ((n) ? wm1 : wm0) + ((a) - 51200) : ((n) ? wr1 : wr0) + (a))
  f32x16 c0 = f32x16{}, c1 = f32x16{};
  bf16x8 xq[8], wg[4];
#define BALPRIO(SL_) __builtin_amdgcn_s_setprio(3 - ((((SL_) >> 1) + 2) & 3))
#define EX(X, i) X[i] = __builtin_amdgcn_exp2f(X[i])
#define HALF(SC, SN, Q0, Q1, OPREV, LPREV, OCUR, LCUR, RD1, RD2, RD3, SL_) do { \
    BALPRIO(SL_); SN = MFMA32(kf0, Q0, nm); SBAR(); RD1; \
    EX(SC, 0); EX(SC, 1); EX(SC, 2); EX(SC, 3); SBAR(); \
    OPREV = MFMA32(vf1, p1, OPREV); asm volatile("" : "+v"(OPREV)); SBAR(); RD2; \
    EX(SC, 4); EX(SC, 5); SBAR(); proj(IC<(SL_) * 2>{}); EX(SC, 6); EX(SC, 7); SBAR(); \
    LPREV = MFMA16(sumA, p1, LPREV); asm volatile("" : "+v"(LPREV)); SBAR(); proj(IC<(SL_) * 2 + 1>{}); \
    p0 = pack8(SC, 0); SBAR(); \
    SN = MFMA32(kf1, Q1, SN); SBAR(); RD3; side(IC<(SL_)>{}); SBAR(); \
    EX(SC, 8); EX(SC, 9); SBAR(); proj(IC<(SL_) * 2 + 2>{}); EX(SC, 10); EX(SC, 11); SBAR(); \
    OCUR = MFMA32(vf0, p0, OCUR); asm volatile("" : "+v"(OCUR)); SBAR(); side(IC<(SL_) + 1>{}); SBAR(); \
    EX(SC, 12); EX(SC, 13); EX(SC, 14); EX(SC, 15); SBAR(); \
    LCUR = MFMA16(sumA, p0, LCUR); asm volatile("" : "+v"(LCUR)); SBAR(); proj(IC<(SL_) * 2 + 3>{}); \
    p1 = pack8(SC, 1); asm volatile("" : "+v"(p1)); SBAR(); \
  } while (0)
#define TILE(vaddr, kaddr, SL_) do { \
    HALF(sA, sB, qb0, qb1, ob, lb, oa, la, vf0 = ldsb128(vaddr), vf1 = ldsb128((vaddr) + 1024), { kf0 = ldsb128(kaddr); kf1 = ldsb128((kaddr) + 1024); }, (SL_)); \
    HALF(sB, sA, qa0, qa1, oa, la, ob, lb, {}, {}, {}, (SL_) + 2); \
  } while (0)
  auto proj = [&](auto P_) __attribute__((always_inline)) {
    constexpr int p = decltype(P_)::value, hx = (p >> 2) - H0, u = p & 3;
    if constexpr (hx >= 1 && hx <= 16) {
      constexpr int k = 2 * (hx - 1) + (u >> 1), now = 2 * (p >> 2) + (u >> 1);
      if constexpr ((u & 1) == 0) { ldx_wait<imin(63, ops_between(x_slot(k) + 1, now))>(xq[k & 7]); c0 = MFMA32(wg[u], xq[k & 7], c0); asm volatile("" : "+v"(c0)); }
      else { c1 = MFMA32(wg[u], xq[k & 7], c1); asm volatile("" : "+v"(c1)); }
      SBAR();
    }
    if constexpr (hx >= 0 && hx < 16) { constexpr unsigned so = w2slot(hx) + (u >> 1) * 2048; if constexpr (hx == 8 && u == 0) asm volatile("" : "+v"(wm0), "+v"(wm1)); wg[u] = ldstr2(WRA(u & 1, so), WRA(u & 1, so) + 1024); SBAR(); }
  };
  u64 fl = 0;
  const char* kvs2 = ws + WS_KVA + (size_t)h * 262144 + grp * 131072 + w4 * 2048;
  unsigned rv[16]; bf16x8 lq0, lq1, lz0, lz1, lk0, lk1; f32x16 den, num;
  auto side = [&](auto SL_) __attribute__((always_inline)) {
    constexpr int sl = decltype(SL_)::value, stg = sl >> 3, i = sl & 7;
    if constexpr (kv_at(sl)) dma16c(l16, kvs2 + (stg + 3) * 8192 + (i - 4) * 512, kvslot((stg + 3) & 3) + wid * 2048 + (i - 4) * 512);
    if constexpr (lin_at(sl) >= 0 && lin_at(sl) < 4) dma16c(l16, ws + WS_KVP + (size_t)(h * 16 + wid * 2) * 2048 + lin_at(sl) * 1024, L_KVP + wid * 4096 + lin_at(sl) * 1024);
    if constexpr (lin_at(sl) >= 4) dma16c(l16, ws + WS_PQB + (size_t)(h * 64 + qb * 8 + wid) * 2048 + (lin_at(sl) - 4) * 1024, L_Q + wid * 2048 + (lin_at(sl) - 4) * 1024);
    if constexpr (lin_at(sl) == 5) dma16c(((r32 & 3) + 4 * (r32 >> 3)) * 64 + hi * 16, ws + WS_ZP + h * 1024 + (wid & 1) * 32, L_Z + (wid & 1) * 1024);
    if constexpr (sl == SL(5, 5)) {
#pragma unroll
      for (int p = 0; p < 16; ++p) rv[p] = *(const LAS unsigned*)(uintptr_t)(L_KVP + p * 2048 + wid * 256 + lane * 4);
    }
    if constexpr (sl == SL(5, 7)) {
      float lo = 0.f, hi_ = 0.f;
#pragma unroll
      for (int p = 0; p < 16; ++p) { lo += __uint_as_float(rv[p] << 16); hi_ += __uint_as_float(rv[p] & 0xffff0000u); }
      *(LAS bf16x2*)(uintptr_t)(L_KVF + wid * 256 + lane * 4) = __builtin_convertvector(f32x2{lo, hi_}, bf16x2);
    }
    if constexpr (sl == SL(6, 1)) { lq0 = ldsb128(L_Q + wid * 2048 + l16); lq1 = ldsb128(L_Q + wid * 2048 + 1024 + l16); lz0 = ldsb128(L_Z + l16); lz1 = ldsb128(L_Z + 1024 + l16); }
    if constexpr (sl == SL(6, 3)) { den = MFMA32(lz0, lq0, f32x16{}); den = MFMA32(lz1, lq1, den); asm volatile("" : "+v"(den)); }
    if constexpr (sl == SL(6, 5)) { lk0 = ldsb128(L_KVF + l16); lk1 = ldsb128(L_KVF + 1024 + l16);
      den[0] = __builtin_amdgcn_rcpf(((((den[0] + den[1]) + (den[2] + den[3])) + ((den[4] + den[5]) + (den[6] + den[7]))) + (((den[8] + den[9]) + (den[10] + den[11])) + ((den[12] + den[13]) + (den[14] + den[15])))) + 1e-6f);
      asm volatile("" : "+v"(den[0])); }
    if constexpr (sl == SL(6, 7)) { num = MFMA32(lk0, lq0, f32x16{}); num = MFMA32(lk1, lq1, num); asm volatile("" : "+v"(num)); }
    if constexpr (sl == SL(7, 1)) { num *= den[0];
      const unsigned oo = WS_O + (4u << 20) + ((qb * 8 + wid) * 64 + 2 * h) * 1024;
      st16(rws, l16, oo, pack8(num, 0)); st16(rws, l16, oo + 1024, pack8(num, 1)); asm volatile("" ::: "memory"); }
    if constexpr (w_at(sl) >= 0) load_w2(w_at(sl));
    if constexpr (sl >= SL(10, 4) && sl < SL(10, 4) + 8) { constexpr int k = sl - SL(10, 4); ldx_async<(k & 1) * 1024>(xq[k & 7], l16, xb2 + (k >> 1) * 4096); }
    if constexpr ((sl & 1) == 0 && sl >= x_slot(8)) {
      constexpr int ko = 8 + (sl - x_slot(8)) - 1, ke = 8 + (sl - x_slot(8));
      if constexpr (ko >= 9 && ko < 32) ldx_async<1024>(xq[ko & 7], l16, xb2 + (ko >> 1) * 4096);
      if constexpr (ke < 32) ldx_async<0>(xq[ke & 7], l16, xb2 + (ke >> 1) * 4096);
    }
  };
  auto stage = [&](auto SC_) __attribute__((always_inline)) {
    constexpr int stg = decltype(SC_)::value;
    const unsigned cur = KVA(stg & 3);
    const unsigned nxt = KVA((stg + 1) & 3);
    TILE(cur + 2048, cur + 4096, stg * 8);
    if (stg < 15) {
      constexpr int now = SL(stg, 4);
      if constexpr (stg == 0) { if (!grp) waitv<4>(); }
      else if constexpr (stg == 1) { if (grp) waitv<0>(); else waitv<2>(); }
      else if constexpr (stg == 8) waitv<0>();
      else if constexpr (stg == 5) waitv<ops_between(SL(4, 4), now)>();
      else if constexpr (stg == 11) waitv<ops_between(SL(10, 3), now)>();
      else if constexpr (stg == 13) waitv<ops_between(SL(12, 5), now)>();
      else if constexpr (stg == 14) waitv<ops_between(SL(13, 5), now)>();
      else waitv<ops_between(SL(stg - 2, 7), now)>();
      if (stg == 3 && wid == 4) { peek_wait<ops_between(SL(2, 4), now)>(fl); await_flags(ws, WS_FB + ((h >> 3) * 64 + lane) * 8, fl, tag); }
      if (stg == 10 && wid == 4) { peek_wait<ops_between(SL(9, 4), now)>(fl); await_flags(ws, WS_FOB + ((m2 >> 1) * 32 + r32) * 8, fl, tag); }
      LBAR();
      if (stg < 4) { if (stg == (idx & 3) && wid < 2) touch((wid ? wob : woa) + (size_t)(bid * 64 + lane) * 128); }
      if (stg == 1 || stg == 2 || stg == 8 || stg == 9) if (wid == 4) {
        if (stg == 1 && lane == 0) post(ws, WS_FB + (nt * 16 + mt) * 8, tag);
        if (stg == 2) peek_async(fl, lane * 8, ws + WS_FB + (h >> 3) * 512);
        if (stg == 8 && lane == 0) post(ws, WS_FOB + (qb * 32 + h) * 8, tag);
        if (stg == 9) peek_async(fl, r32 * 8, ws + WS_FOB + (m2 >> 1) * 256);
      }
      SBAR();
    }
    TILE(cur + 4096 + 2048, nxt, stg * 8 + 4);
  };
#define STG(n) stage(IC<n>{})
  STG(0); STG(1); STG(2); STG(3); STG(4); STG(5); STG(6); STG(7); STG(8); STG(9); STG(10); STG(11); STG(12); STG(13); STG(14); STG(15);
  ob = MFMA32(vf1, p1, ob); lb = MFMA16(sumA, p1, lb);
  __builtin_amdgcn_s_setprio(0);
  { LAS f32x4* ex = (LAS f32x4*)(uintptr_t)((grp ? kvslot(1) : kvslot(0)) + w4 * 4096);
    const f32x16& so = ob;
#pragma unroll
    for (int g = 0; g < 4; ++g) ex[g * 64 + lane] = f32x4{so[4 * g], so[4 * g + 1], so[4 * g + 2], so[4 * g + 3]};
    ((LAS f32x2*)(uintptr_t)(L_LM + wid * 512))[lane] = f32x2{lb[0], nm[0]}; }
  LBAR();
#pragma unroll
  for (int t = 23; t < 27; ++t) load_w2(t);
  auto g2_put = [&]() __attribute__((always_inline)) {
    LAS f32x4* ex = (LAS f32x4*)(uintptr_t)((grp ? kvslot(3) : kvslot(2)) + w4 * 4096);
    const f32x16& so = c1;
#pragma unroll
    for (int g = 0; g < 4; ++g) ex[g * 64 + lane] = f32x4{so[4 * g], so[4 * g + 1], so[4 * g + 2], so[4 * g + 3]};
  };
  auto g2_get = [&]() __attribute__((always_inline)) {
    const LAS f32x4* rx = (const LAS f32x4*)(uintptr_t)((grp ? kvslot(2) : kvslot(3)) + w4 * 4096);
    f32x16 a = c0;
#pragma unroll
    for (int g = 0; g < 4; ++g) { const f32x4 e = rx[g * 64 + lane]; a[4 * g] += e[0]; a[4 * g + 1] += e[1]; a[4 * g + 2] += e[2]; a[4 * g + 3] += e[3]; }
    const unsigned tb = (grp ? kvslot(2) : kvslot(3)) + w4 * 4096 + r32 * 64;
    asm volatile("s_waitcnt lgkmcnt(0)" ::: "memory");
    *(LAS bf16x8*)(uintptr_t)(tb + ((hi ^ ((r32 >> 1) & 3)) * 16)) = pack8(a, 0);
    *(LAS bf16x8*)(uintptr_t)(tb + (((2 + hi) ^ ((r32 >> 1) & 3)) * 16)) = pack8(a, 1);
  };
  auto g2_store = [&](int prob) __attribute__((always_inline)) {
#pragma unroll
    for (int i = 0; i < 2; ++i) {
      const int row = grp * 16 + i * 8 + (lane >> 3), c = lane & 7;
      const bf16x8 v = ldsb128(((c >> 2) ? kvslot(2) : kvslot(3)) + w4 * 4096 + row * 64 + (((c & 3) ^ ((row >> 1) & 3)) * 16));
      __builtin_nontemporal_store(__builtin_bit_cast(u32x4, v), (u32x4*)(out + (size_t)(m2 * 128 + w4 * 32 + row) * 2048 + prob * 1024 + n2 * 64 + c * 8));
    }
  };
  {
    f32x16 o = oa;
    float l = la[0];
    const LAS f32x4* ex = (const LAS f32x4*)(uintptr_t)((grp ? kvslot(0) : kvslot(1)) + w4 * 4096);
    const f32x2 e = ((const LAS f32x2*)(uintptr_t)(L_LM + (wid ^ 4) * 512))[lane];
    const float n1 = nm[0], n2_ = e[1], nn = __builtin_fminf(n1, n2_);
    const float f1 = __builtin_amdgcn_exp2f(nn - n1), f2 = __builtin_amdgcn_exp2f(nn - n2_);
    bool bad = !(l < 1.0e19f) || !(e[0] < 1.0e19f) || !(l > 1.0e-12f) || !(e[0] > 1.0e-12f);
    l = l * f1 + e[0] * f2;
#pragma unroll
    for (int g = 0; g < 4; ++g) { const f32x4 x = ex[g * 64 + lane];
#pragma unroll
      for (int j = 0; j < 4; ++j) o[4 * g + j] = o[4 * g + j] * f1 + x[j] * f2; }
    { const f32x8 s8 = __builtin_shufflevector(o, o, 0, 1, 2, 3, 4, 5, 6, 7) + __builtin_shufflevector(o, o, 8, 9, 10, 11, 12, 13, 14, 15);
      const f32x4 s4 = __builtin_shufflevector(s8, s8, 0, 1, 2, 3) + __builtin_shufflevector(s8, s8, 4, 5, 6, 7);
      bad |= !(__builtin_fabsf((s4[0] + s4[2]) + (s4[1] + s4[3])) < 3.0e38f); }
    if (__builtin_expect(__any(bad), 0)) {
      const unsigned g = WS_KVA + h * 262144;
      const bf16x8 q0 = qa0, q1 = qa1;
      float m = -INFINITY; l = 0.f; o = f32x16{};
      for (int t = 0; t < 64; ++t) {
        const bf16x8 k0 = ld16(rws, l16, g + t * 4096), k1 = ld16(rws, l16 + 1024, g + t * 4096);
        const bf16x8 v0 = ld16(rws, l16 + 2048, g + t * 4096), v1 = ld16(rws, l16 + 3072, g + t * 4096);
        f32x16 s = MFMA32(k0, q0, f32x16{}); s = MFMA32(k1, q1, s);
        const float mn = __builtin_fmaxf(m, rowmax16(s));
        const float al = __builtin_amdgcn_exp2f(m - mn);
        m = mn;
#pragma unroll
        for (int r = 0; r < 16; ++r) s[r] = __builtin_amdgcn_exp2f(s[r] - mn);
        const bf16x8 a0 = pack8(s, 0), a1 = pack8(s, 1);
        f32x4 ls = MFMA16(sumA, a0, f32x4{}); ls = MFMA16(sumA, a1, ls);
        o *= al; l = l * al + ls[0];
        o = MFMA32(v0, a0, o); o = MFMA32(v1, a1, o);
      }
    }
    o *= __builtin_amdgcn_rcpf(l);
    const unsigned oo = WS_O + ((qb * 8 + w4 * 2 + grp) * 64 + 2 * h) * 1024;
    st16(rws, l16, oo, pack8(o, 0)); st16(rws, l16, oo + 1024, pack8(o, 1));
  }
  SBAR();
  bf16x8 xr[16][2], wf[2][4];
#define LDX(R, base, t) do { R[0] = ld16(rws, l16, (base) + (t) * 4096); R[1] = ld16(rws, l16 + 1024, (base) + (t) * 4096); } while (0)
#define RDW2(b, t) do { const unsigned so = w2slot(t); \
    wf[b][0] = ldstr2(WRA(0, so), WRA(0, so) + 1024); wf[b][1] = ldstr2(WRA(1, so), WRA(1, so) + 1024); \
    wf[b][2] = ldstr2(WRA(0, so) + 2048, WRA(0, so) + 3072); wf[b][3] = ldstr2(WRA(1, so) + 2048, WRA(1, so) + 3072); } while (0)
#define MM2(b, X0, X1) do { c0 = MFMA32(wf[b][0], X0, c0); c1 = MFMA32(wf[b][1], X0, c1); c0 = MFMA32(wf[b][2], X1, c0); c1 = MFMA32(wf[b][3], X1, c1); } while (0)
  g2_put();
  asm volatile("s_waitcnt vmcnt(0) lgkmcnt(0)\n\ts_barrier" ::: "memory");
  if (tid == 0) post(ws, WS_FOA + (qb * 32 + h) * 8, tag);
#pragma unroll
  for (int t = 27; t < 32; ++t) load_w2(t);
  g2_get();
  LBAR();
  g2_store(1);
  if (wid == 4) await_flags(ws, WS_FOA + ((m2 >> 1) * 32 + r32) * 8, peek(ws, WS_FOA + ((m2 >> 1) * 32 + r32) * 8), tag);
  asm volatile("s_waitcnt vmcnt(2) lgkmcnt(0)\n\ts_barrier" ::: "memory");
  __builtin_amdgcn_s_setprio(3);
  LDX(xr[0], xo, 0); LDX(xr[1], xo, 1);
  if (bid == 0 && tid == 256) post(ws, WS_EPOCH, tag);
  c0 = f32x16{}; c1 = f32x16{};
  RDW2(0, 16);
#pragma unroll
  for (int t = 0; t < 16; ++t) {
    if (t == 0) __builtin_amdgcn_s_setprio(1); if (t == 5) __builtin_amdgcn_s_setprio(0);
    if (t + 2 < 16) LDX(xr[t + 2], xo, t + 2);
    if (t < 15) RDW2((t + 1) & 1, 16 + t + 1);
    SBAR(); MM2(t & 1, xr[t][0], xr[t][1]); SBAR();
  }
  g2_put();
  LBAR();
  g2_get();
  LBAR();
  g2_store(0);
}

extern "C" void kernel_launch(void* const* d_in, const int* in_sizes, int n_in, void* d_out, int out_size, void* d_ws, size_t ws_size, hipStream_t stream) {
  (void)hipFuncSetAttribute((const void*)k_fused, hipFuncAttributeMaxDynamicSharedMemorySize, LDS_BYTES);
  k_fused<<<256, 512, LDS_BYTES, stream>>>((const char*)d_in[0], (const char*)d_in[1], (const char*)d_in[2], (const char*)d_in[3], (const char*)d_in[4], (const char*)d_in[5],
                                           (u16*)d_out, (char*)d_ws);
}
```
